```python
import torch

def _softmax(x, axis=-1):
    return torch.softmax(x, dim=axis)

def _gelu(x):
    return torch.nn.functional.gelu(x, approximate='tanh')

def _silu(x):
    return torch.nn.functional.silu(x)

def _sigmoid(x):
    return torch.sigmoid(x)

def _layernorm(x, axis=-1, eps=1e-05):
    m = x.mean(dim=axis, keepdim=True)
    v = x.var(dim=axis, keepdim=True, unbiased=False)
    return (x - m) / torch.sqrt(v + eps)

def _scatter_add(tgt, idx, src, axis=0):
    return tgt.clone().index_add_(axis, idx, src)

def _segment_sum(src, seg, n):
    out = torch.zeros((n,) + src.shape[1:], dtype=src.dtype)
    return out.index_add_(0, seg, src)

def _pad(x, pw):
    flat = tuple((v for pair in reversed(pw) for v in pair))
    return torch.nn.functional.pad(x, flat)
kernel_name = 'recurrent_scan__memory_aug_recurrent_mem_tr__mixer_channel_mlp__prompt'

def setup_inputs():

    _w = lambda a: (lambda t: t.to(torch.bfloat16) if t.is_floating_point() else t)(torch.as_tensor(a))

    def _A_setup():
        B, S, Dm = (1, 2048, 640)
        H, Dh = (32, 192)
        M, seg = (8, 32)
        seg = min(seg, S)
        S = S // seg * seg
        return {'x': _w((lambda *s: torch.randn(*s, dtype=torch.bfloat16) * 0.05)(B, S, Dm)), 'mem0': _w((lambda *s: torch.randn(*s, dtype=torch.bfloat16) * 0.8)(B, M, Dm)), 'Wq': _w((lambda *s: torch.randn(*s, dtype=torch.bfloat16) * 0.8)(Dm, H, Dh) * Dm ** (-0.5)), 'Wk': _w((lambda *s: torch.randn(*s, dtype=torch.bfloat16) * 0.8)(Dm, H, Dh) * Dm ** (-0.5)), 'Wv': _w((lambda *s: torch.randn(*s, dtype=torch.bfloat16) * 0.8)(Dm, H, Dh) * Dm ** (-0.5)), 'Wo': _w((lambda *s: torch.randn(*s, dtype=torch.bfloat16) * 0.8)(H, Dh, Dm) * (H * Dh) ** (-0.5)), 'seg_len': seg}
    _A_in = _A_setup()

    def _B_setup():
        B, S, D, F = (1, 2048, 640, 1536)
        return {'x': _w((lambda *s: torch.randn(*s, dtype=torch.bfloat16) * 0.05)(B, S, D)), 'w1': _w((lambda *s: torch.randn(*s, dtype=torch.bfloat16) * 1.6)(D, F) * D ** (-0.5)), 'b1': _w((lambda *s: torch.randn(*s, dtype=torch.bfloat16) * 0.4)(F)), 'w2': _w((lambda *s: torch.randn(*s, dtype=torch.bfloat16) * 0.2)(F, D) * F ** (-0.5)), 'b2': _w((lambda *s: torch.randn(*s, dtype=torch.bfloat16) * 0.2)(D))}
    _B_in = _B_setup()
    return {'A_x': _A_in['x'], 'A_mem0': _A_in['mem0'], 'A_Wq': _A_in['Wq'], 'A_Wk': _A_in['Wk'], 'A_Wv': _A_in['Wv'], 'A_Wo': _A_in['Wo'], 'A_seg_len': _A_in['seg_len'], 'B_x': _B_in['x'], 'B_w1': _B_in['w1'], 'B_b1': _B_in['b1'], 'B_w2': _B_in['w2'], 'B_b2': _B_in['b2']}

def reference(A_x, A_mem0, A_Wq, A_Wk, A_Wv, A_Wo, A_seg_len, B_x, B_w1, B_b1, B_w2, B_b2):

    def _A_ref(x, mem0, Wq, Wk, Wv, Wo, seg_len):
        B, S, Dm = x.shape
        H, Dh = (Wq.shape[1], Wq.shape[2])
        M = mem0.shape[1]
        scale = Dh ** (-0.5)
        n_seg = S // seg_len
        L = M + seg_len
        idx = torch.arange(L)
        mask = (idx[:, None] < M) | (idx[:, None] >= idx[None, :])
        mem = mem0
        outs = []
        for s in range(n_seg):
            xs = x[:, s * seg_len:(s + 1) * seg_len, :].clone()
            h = torch.concatenate([mem, xs], axis=1)
            q = torch.einsum('btd,dhe->bhte', h, Wq)
            k = torch.einsum('btd,dhe->bhte', h, Wk)
            v = torch.einsum('btd,dhe->bhte', h, Wv)
            scores = torch.einsum('bhqe,bhke->bhqk', q, k) * scale
            scores = torch.where(mask[None, None], scores, -1000000000.0)
            p = _softmax(scores, axis=-1)
            o = torch.einsum('bhqk,bhke->bhqe', p, v)
            o = torch.einsum('bhte,hed->btd', o, Wo)
            mem = o[:, :M, :].clone()
            outs.append(o[:, M:, :])
        return torch.concatenate(outs, axis=1)

    def _B_ref(x, w1, b1, w2, b2):
        y = _layernorm(x, axis=-1)
        h = _gelu(torch.einsum('bsd,df->bsf', y, w1) + b1)
        return torch.einsum('bsf,fd->bsd', h, w2) + b2
    x = A_x
    xn = _layernorm(x, axis=-1)
    s = _A_ref(xn, A_mem0, A_Wq, A_Wk, A_Wv, A_Wo, A_seg_len)
    g = _silu(_B_ref(xn, B_w1, B_b1, B_w2, B_b2))
    return x + s * g
```

```cpp
#include <hip/hip_runtime.h>
#include <hip/hip_bf16.h>
#include <stdint.h>
#include <stdlib.h>

typedef unsigned v4u __attribute__((ext_vector_type(4)));
typedef unsigned v2u __attribute__((ext_vector_type(2)));
typedef unsigned v8u __attribute__((ext_vector_type(8)));
typedef float f4 __attribute__((ext_vector_type(4)));
typedef float f2 __attribute__((ext_vector_type(2)));
typedef __bf16 bf2 __attribute__((ext_vector_type(2)));
typedef __bf16 bf8 __attribute__((ext_vector_type(8)));
typedef __bf16 bf16v __attribute__((ext_vector_type(16)));
typedef short s4 __attribute__((ext_vector_type(4)));
typedef unsigned char u8;
#define LAS __attribute__((address_space(3)))
#define DEVI __device__ __forceinline__

constexpr int NSEG = 64;
constexpr unsigned SENT = 0xFFFFFFFFu;
constexpr unsigned SPIN_CAP = 1u << 17;

constexpr unsigned L_MEM = 0, L_MEM_REP = 16384, L_MEM_SLOT = 65536;
constexpr unsigned L_CLK = 196608;
constexpr unsigned L_Y01 = 204800, L_Y_SLOT = 327680;
constexpr unsigned L_P = 327680, L_P_REP = 24576, L_P_SLOT = 98304;
constexpr unsigned WIN = 2u << 20;
constexpr unsigned O_CTL = 2 * WIN;
constexpr unsigned PW = 128;
constexpr unsigned C_CLS = O_CTL;
constexpr unsigned C_PROGP = O_CTL + 4096;
constexpr unsigned C_PROGU = C_PROGP + 320 * PW;
constexpr unsigned C_PROGG = C_PROGU + 128 * PW;
constexpr unsigned C_PROGY = C_PROGG + 64 * PW;
constexpr unsigned C_PROGQ = C_PROGY + 160 * PW;
constexpr unsigned C_TICK = C_PROGQ + 32 * PW;
constexpr unsigned C_END = C_TICK + 20 * PW;
constexpr unsigned CTL_BYTES = 262144;
static_assert(C_END <= O_CTL + CTL_BYTES, "control block");
constexpr unsigned O_PX = O_CTL + CTL_BYTES, PX_SLOT = 81920;
constexpr unsigned O_YC = O_PX + NSEG * PX_SLOT;
constexpr unsigned O_XN = O_YC + NSEG * 327680;
constexpr unsigned O_Y2 = O_XN + NSEG * 40960;
constexpr unsigned O_QX = O_Y2 + NSEG * 40960;
constexpr unsigned QKV_B = NSEG * 32 * 12288;
constexpr unsigned O_KX = O_QX + QKV_B;
constexpr unsigned O_VX = O_KX + QKV_B;
constexpr unsigned O_H1 = O_VX + QKV_B;
constexpr unsigned O_G = O_H1 + NSEG * 98304;
constexpr unsigned O_U = O_G + NSEG * 40960;
constexpr unsigned WS_USED = O_U + NSEG * 40u * 32768u;
static_assert(WS_USED + WIN <= (256u << 20), "workspace");

constexpr int LDS_BYTES = 160 * 1024;

struct Params {
  const u8 *x, *mem0, *wq, *wk, *wv, *wo, *w1, *b1, *w2, *b2;
  u8* out;
  u8* ws;
};

constexpr unsigned OOB = 0xF0000000u;
#define EV(i)
constexpr unsigned RS_BYTES = WS_USED;
#define ST_DECL unsigned long long* st_ = nullptr;
#define ST(i)
#define ST_FLUSH(slot, s)
#define CT_DECL
#define CT(i)
#define CT_FLUSH(slot, s)
typedef __amdgpu_buffer_rsrc_t rsrc_t;
DEVI v4u ld16(rsrc_t r, unsigned off) { return __builtin_amdgcn_raw_buffer_load_b128(r, off, 0, 16); }
DEVI v2u ld8(rsrc_t r, unsigned off) { return __builtin_amdgcn_raw_buffer_load_b64(r, off, 0, 16); }
DEVI unsigned ld4(rsrc_t r, unsigned off) { return __builtin_amdgcn_raw_buffer_load_b32(r, off, 0, 16); }
DEVI void st16(rsrc_t r, unsigned off, v4u v) { __builtin_amdgcn_raw_buffer_store_b128(v, r, off, 0, 16); }
DEVI void st8(rsrc_t r, unsigned off, v2u v) { __builtin_amdgcn_raw_buffer_store_b64(v, r, off, 0, 16); }
DEVI void st4(rsrc_t r, unsigned off, unsigned v) { __builtin_amdgcn_raw_buffer_store_b32(v, r, off, 0, 16); }
DEVI unsigned amap(unsigned x, unsigned cls8k) { return ((x >> 13) << 14) + (x & 8191u) + cls8k; }
DEVI unsigned pk(float a, float b) { bf2 r = {(__bf16)a, (__bf16)b}; return __builtin_bit_cast(unsigned, r); }
DEVI unsigned san(unsigned v) { return v < 0xFFFFFFFEu ? v : 0xFFFFFFFEu; }
DEVI float blo(unsigned v) { return __uint_as_float(v << 16); }
DEVI float bhi(unsigned v) { return __uint_as_float(v & 0xFFFF0000u); }
DEVI float rbf(float a) { return blo(pk(a, a)); }
DEVI unsigned umax3(unsigned a, unsigned b, unsigned c) { return max(max(a, b), c); }
DEVI unsigned chk(unsigned m, v4u v) { return umax3(umax3(m, v.x, v.y), v.z, v.w); }
DEVI unsigned chk(unsigned m, v2u v) { return umax3(m, v.x, v.y); }
DEVI bool all_valid(unsigned m) { asm volatile("" : "+v"(m)); return __builtin_amdgcn_ballot_w64(m == SENT) == 0; }
DEVI f4 mfma(v4u a, v4u b, f4 c) { return __builtin_amdgcn_mfma_f32_16x16x32_bf16(__builtin_bit_cast(bf8, a), __builtin_bit_cast(bf8, b), c, 0, 0, 0); }
DEVI f4 smf(v4u a, v8u b, f4 c, int idx) { return __builtin_amdgcn_smfmac_f32_16x16x64_bf16(__builtin_bit_cast(bf8, a), __builtin_bit_cast(bf16v, b), c, idx, 0, 0); }
DEVI v8u cat(v4u a, v4u b) { return __builtin_shufflevector(a, b, 0, 1, 2, 3, 4, 5, 6, 7); }
DEVI v4u cat(v2u a, v2u b) { return __builtin_shufflevector(a, b, 0, 1, 2, 3); }
DEVI v4u lo4(v8u a) { return __builtin_shufflevector(a, a, 0, 1, 2, 3); }
DEVI v4u hi4(v8u a) { return __builtin_shufflevector(a, a, 4, 5, 6, 7); }
DEVI v4u lr16(unsigned a) { return *(LAS v4u*)(uintptr_t)a; }
DEVI f4 lrf4(unsigned a) { return *(LAS f4*)(uintptr_t)a; }
DEVI f2 lrf2(unsigned a) { return *(LAS f2*)(uintptr_t)a; }
DEVI void lw16(unsigned a, v4u v) { *(LAS v4u*)(uintptr_t)a = v; }
DEVI void lwf4(unsigned a, f4 v) { *(LAS f4*)(uintptr_t)a = v; }
DEVI void lw8(unsigned a, v2u v) { *(LAS v2u*)(uintptr_t)a = v; }
DEVI void lw4(unsigned a, unsigned v) { *(LAS unsigned*)(uintptr_t)a = v; }
DEVI void lwf(unsigned a, float v) { *(LAS float*)(uintptr_t)a = v; }
DEVI v2u trd(unsigned a) { return __builtin_bit_cast(v2u, __builtin_amdgcn_ds_read_tr16_b64_v4i16((LAS s4*)(uintptr_t)a)); }
DEVI void bar() { asm volatile("s_waitcnt lgkmcnt(0)\n\ts_barrier" ::: "memory"); }
#define WAITV(n) asm volatile("s_waitcnt vmcnt(%0)" ::"i"(n) : "memory")
#define FENCE() asm volatile("" ::: "memory")
#define SB() __builtin_amdgcn_sched_barrier(0)
#define PINA(x) asm volatile("" : "+a"(x))
#define PINV(x) asm volatile("" : "+v"(x))
#define RAWBAR() __builtin_amdgcn_s_barrier()
#define WAITL0() asm volatile("s_waitcnt lgkmcnt(0)" ::: "memory")
#define WPOL " nt"
DEVI void dma(const u8* base, unsigned voff, unsigned ldsaddr) {
  unsigned keep;
  asm volatile("s_mov_b32 %0, m0\n\ts_mov_b32 m0, %3\n\ts_nop 2\n\tglobal_load_lds_dwordx4 %1, %2" WPOL "\n\ts_mov_b32 m0, %0"
               : "=&s"(keep) : "v"(voff), "s"(base), "s"(__builtin_amdgcn_readfirstlane(ldsaddr)) : "memory");
}
DEVI void dma_sc1(const u8* base, unsigned voff, unsigned ldsaddr) {
  unsigned keep;
  asm volatile("s_mov_b32 %0, m0\n\ts_mov_b32 m0, %3\n\ts_nop 2\n\tglobal_load_lds_dwordx4 %1, %2 sc1\n\ts_mov_b32 m0, %0"
               : "=&s"(keep) : "v"(voff), "s"(base), "s"(__builtin_amdgcn_readfirstlane(ldsaddr)) : "memory");
}
DEVI void dma_words_sc1(const u8* base, unsigned voff, unsigned ldsaddr) {
  unsigned keep;
  asm volatile("s_mov_b32 %0, m0\n\ts_mov_b32 m0, %3\n\ts_nop 2\n\tglobal_load_lds_dword %1, %2 sc1\n\ts_mov_b32 m0, %0"
               : "=&s"(keep) : "v"(voff), "s"(base), "s"(__builtin_amdgcn_readfirstlane(ldsaddr)) : "memory");
}
DEVI void dma2(const u8* base, unsigned voff, unsigned ldsaddr) {
  unsigned keep;
  asm volatile("s_mov_b32 %0, m0\n\ts_mov_b32 m0, %3\n\ts_nop 2\n\tglobal_load_lds_dwordx4 %1, %2 sc1\n\tglobal_load_lds_dwordx4 %1, %2 offset:1024 sc1\n\ts_mov_b32 m0, %0"
               : "=&s"(keep) : "v"(voff), "s"(base), "s"(__builtin_amdgcn_readfirstlane(ldsaddr)) : "memory");
}
DEVI void dma3_sc1(const u8* base, unsigned voff, unsigned ldsaddr) {
  unsigned keep;
  asm volatile("s_mov_b32 %0, m0\n\ts_mov_b32 m0, %3\n\ts_nop 2\n\tglobal_load_lds_dwordx4 %1, %2 sc1\n\tglobal_load_lds_dwordx4 %1, %2 offset:1024 sc1\n\tglobal_load_lds_dwordx4 %1, %2 offset:2048 sc1\n\ts_mov_b32 m0, %0"
               : "=&s"(keep) : "v"(voff), "s"(base), "s"(__builtin_amdgcn_readfirstlane(ldsaddr)) : "memory");
}
DEVI float swapadd32(float a, float b) {
  auto r = __builtin_amdgcn_permlane32_swap(__float_as_uint(a), __float_as_uint(b), false, false);
  return __uint_as_float(r[0]) + __uint_as_float(r[1]);
}
DEVI float x32max(float a) { auto r = __builtin_amdgcn_permlane32_swap(__float_as_uint(a), __float_as_uint(a), false, false); return fmaxf(__uint_as_float(r[0]), __uint_as_float(r[1])); }
DEVI float x16max(float a) { auto r = __builtin_amdgcn_permlane16_swap(__float_as_uint(a), __float_as_uint(a), false, false); return fmaxf(__uint_as_float(r[0]), __uint_as_float(r[1])); }
DEVI float x32add(float a) { auto r = __builtin_amdgcn_permlane32_swap(__float_as_uint(a), __float_as_uint(a), false, false); return __uint_as_float(r[0]) + __uint_as_float(r[1]); }
DEVI float x16add(float a) { auto r = __builtin_amdgcn_permlane16_swap(__float_as_uint(a), __float_as_uint(a), false, false); return __uint_as_float(r[0]) + __uint_as_float(r[1]); }
DEVI float ror8(float a) { return __int_as_float(__builtin_amdgcn_update_dpp(0, __float_as_int(a), 0x128, 0xF, 0xF, false)); }
DEVI float ex2(float a) { return __builtin_amdgcn_exp2f(a); }
DEVI float rcp(float a) { return __builtin_amdgcn_rcpf(a); }

template <int I> struct IC { static constexpr int v = I; };
template <int I, int N, class F> DEVI void sfor(F&& f) { if constexpr (I < N) { f(IC<I>{}); sfor<I + 1, N>(f); } }

DEVI void wait_prog(rsrc_t rs, unsigned o, int n, int s, int& have, bool& dead, int lane) {
  if (have > s || dead) return;
  const unsigned off = o + PW * (unsigned)(lane < n ? lane : 0);
  for (unsigned it = 0;; ++it) {
    FENCE();
    int v = (int)ld4(rs, off);
    if (__builtin_amdgcn_ballot_w64(v <= s) == 0) {
      have = s + 1;
      if (__builtin_amdgcn_ballot_w64(v < s + 2) == 0) have = s + 2;
      if (__builtin_amdgcn_ballot_w64(v < s + 4) == 0) have = s + 4;
      if (__builtin_amdgcn_ballot_w64(v < s + 8) == 0) have = s + 8;
      if (__builtin_amdgcn_ballot_w64(v < s + 32) == 0) have = s + 32;
      return;
    }
    if (it > SPIN_CAP) { dead = true; return; }
    __builtin_amdgcn_s_sleep(4);
  }
}

DEVI float red4(float v) { return x32add(x16add(v)); }
__global__ void __launch_bounds__(640) k_pre(Params p) {
  __shared__ float red[4][16][12];
  const int t = threadIdx.x;
  if (blockIdx.x < 64) {
    const int b = blockIdx.x;
    if ((b & 7) >= 4 || t != 0) return;
    rsrc_t rs = __builtin_amdgcn_make_buffer_rsrc(p.ws, 0, WS_USED, 0x00020000);
    const unsigned win = (b >> 3) & 1, cl = (b >> 4) & 1, u = (b >> 5) * 4 + (b & 3);
    unsigned mn = 0xFFFFFFFFu;
    const unsigned off = win * WIN + WIN - 16384u + cl * 8192u + u * 1024u;
    for (int k = 0; k < 5; ++k) {
      unsigned long long c0 = __builtin_amdgcn_s_memtime();
      asm volatile("s_waitcnt lgkmcnt(0)" ::: "memory");
      st4(rs, off, 1u);
      WAITV(0);
      unsigned long long c1 = __builtin_amdgcn_s_memtime();
      unsigned d = (unsigned)(c1 - c0);
      mn = d < mn ? d : mn;
    }
    *(unsigned*)(p.ws + C_CLS + ((win * 2 + cl) * 8 + u) * 4) = mn;
    return;
  }
  {
    const int b = blockIdx.x - 64, lane = t & 63, w = t >> 6, g = lane >> 4, m = lane & 15;
    const v4u* xr = (const v4u*)(p.x + (size_t)(b * 16 + m) * 1280);
    const v4u c0 = xr[w * 8 + g], c1 = xr[w * 8 + 4 + g];
#pragma unroll
    for (int k = 0; k < 3; ++k) {
      const unsigned pc = b * 1536u + k * 640u + t, ib = pc >> 10, o = (pc & 1023u) * 16u;
      if (k * 640 + t < 1536) {
        v4u val = {SENT, SENT, SENT, SENT};
        unsigned off = (ib < 105 ? ib * 16384u : WIN + (ib - 105) * 16384u) + o;
        if (ib < 8) {
          const unsigned lx = (ib & 1) * 8192u + (o & 8191u);
          if (lx < 10240u) {
            const unsigned c = lx >> 8, mm = (lx >> 4) & 15, half = (mm >> 1) & 1, i = 2 * (mm >> 2) + (mm & 1);
            const v4u mv = *(const v4u*)(p.mem0 + i * 1280 + c * 32 + half * 16);
            val = (v4u){san(mv.x), san(mv.y), san(mv.z), san(mv.w)};
          }
        }
        if (ib >= 181) { off = O_CTL + 4096u + (ib - 181) * 16384u + o; val = (v4u){0, 0, 0, 0}; }
        *(v4u*)(p.ws + off) = val;
      }
    }
    float v[16];
#pragma unroll
    for (int j = 0; j < 4; ++j) { v[2 * j] = blo(c0[j]); v[2 * j + 1] = bhi(c0[j]); v[8 + 2 * j] = blo(c1[j]); v[9 + 2 * j] = bhi(c1[j]); }
#pragma unroll
    for (int pass = 0; pass < 2; ++pass) {
      float s = 0.f;
#pragma unroll
      for (int i = 0; i < 16; ++i) s += v[i];
      red[2 * pass][m][w] = red4(s);
      __syncthreads();
      float mean = 0.f;
#pragma unroll
      for (int i = 0; i < 10; ++i) mean += red[2 * pass][m][i];
      mean *= (1.f / 640.f);
      float q = 0.f;
#pragma unroll
      for (int i = 0; i < 16; ++i) { const float d = v[i] - mean; q += d * d; }
      red[2 * pass + 1][m][w] = red4(q);
      __syncthreads();
      float var = 0.f;
#pragma unroll
      for (int i = 0; i < 10; ++i) var += red[2 * pass + 1][m][i];
      var *= (1.f / 640.f);
      const float mb = rbf(mean);
      const float rinv = 1.f / rbf(sqrtf(rbf(rbf(var) + 1e-5f)));
      u8* dst = p.ws + (pass == 0 ? O_XN : O_Y2) + (size_t)(b >> 1) * 40960 + w * 4096 + (g >> 1) * 1024 + ((g & 1) * 32 + (b & 1) * 16 + m) * 16;
#pragma unroll
      for (int k = 0; k < 2; ++k) {
        v4u o;
#pragma unroll
        for (int j = 0; j < 4; ++j) {
          const unsigned d = pk(v[k * 8 + 2 * j] - mb, v[k * 8 + 2 * j + 1] - mb);
          const unsigned r = pk(blo(d) * rinv, bhi(d) * rinv);
          o[j] = r; v[k * 8 + 2 * j] = blo(r); v[k * 8 + 2 * j + 1] = bhi(r);
        }
        *(v4u*)(dst + k * 2048) = o;
      }
    }
  }
}
static_assert(L_Y01 + 2 * L_Y_SLOT <= 105 * 8192 && L_P + 3 * L_P_SLOT <= 76 * 8192 && C_END <= O_CTL + 4096 + 11 * 16384, "init coverage");

struct Ctx {
  rsrc_t rs;
  unsigned lb;
  int lane, w, g, n;
  int x;
  int k1, q, ka, kb, kw, dec, kd, kv;
};

typedef float f16v __attribute__((ext_vector_type(16)));
DEVI f16v mfma32(v4u a, v4u b, f16v c) { return __builtin_amdgcn_mfma_f32_32x32x16_bf16(__builtin_bit_cast(bf8, a), __builtin_bit_cast(bf8, b), c, 0, 0, 0); }
template <bool MLP>
DEVI void role_P(const Params& p, const Ctx& c, int cbase) {
  const int lane = c.lane, w = c.w, g = c.g;
  const int cb = cbase + w;
  const u8* W; unsigned ldw, col0, obase, ostride;
  if (MLP) { W = p.w1; ldw = 3072; col0 = (cb - 288) * 128; obase = O_H1 + (cb - 288) * 2048; ostride = 98304; }
  else {
    const int kind = cb / 96, r = cb % 96;
    W = kind == 0 ? p.wq : kind == 1 ? p.wk : p.wv; ldw = 12288; col0 = r * 128;
    obase = O_QX + kind * QKV_B + (r / 3) * 12288 + (r % 3) * 2048; ostride = 32 * 12288;
  }
  const unsigned rt_stride = MLP ? 49152u : 6144u;
  v4u A[2][40];
  {
    const unsigned st = c.lb + w * 32768;
    const unsigned voff = (lane >> 3) * ldw + col0 + (lane & 7) * 16;
    const unsigned q = (lane >> 2) & 3, pp = lane & 3;
    const unsigned ra = st + (8 * (g >> 1) + q) * 128 + (16 * (pp & 1) + 4 * (2 * (g & 1) + (pp >> 1))) * 2;
#pragma unroll
    for (int ch = 0; ch < 4; ++ch)
#pragma unroll
      for (int i = 0; i < 8; ++i) dma(W + (size_t)(ch * 64 + i * 8) * ldw, voff, st + ch * 8192 + i * 1024);
#pragma unroll
    for (int ch = 0; ch < 10; ++ch) {
      if (ch <= 6) WAITV(24); else if (ch == 7) WAITV(16); else if (ch == 8) WAITV(8); else WAITV(0);
#pragma unroll
      for (int kk = 0; kk < 4; ++kk)
#pragma unroll
        for (int t = 0; t < 2; ++t) {
          const unsigned a = ra + (ch & 3) * 8192 + kk * 2048 + t * 64;
          A[t][ch * 4 + kk] = cat(trd(a), trd(a + 512));
          if (ch < (MLP ? 5 : 6)) PINA(A[t][ch * 4 + kk]); else PINV(A[t][ch * 4 + kk]);
        }
      if (ch + 4 < 10) {
        WAITL0();
#pragma unroll
        for (int i = 0; i < 8; ++i) dma(W + (size_t)((ch + 4) * 64 + i * 8) * ldw, voff, st + (ch & 3) * 8192 + i * 1024);
      }
    }
  }
  f16v bias[2];
#pragma unroll
  for (int t = 0; t < 2; ++t)
#pragma unroll
    for (int u = 0; u < 2; ++u) {
      v4u bv = {0, 0, 0, 0};
      if (MLP) bv = *(const v4u*)(p.b1 + col0 + t * 64 + (lane >> 5) * 32 + u * 16);
#pragma unroll
      for (int j = 0; j < 4; ++j) { bias[t][8 * u + 2 * j] = blo(bv[j]); bias[t][8 * u + 2 * j + 1] = bhi(bv[j]); }
    }
  if (MLP) { PINA(bias[0]); PINA(bias[1]); }
  EV(1);
  WAITV(0);
  bar();
  const u8* X = p.ws + (MLP ? O_Y2 : O_XN) + w * 10240;
  unsigned xv = lane * 16;
  const unsigned xl = c.lb + w * 10240;
  unsigned progo = lane == 0 ? C_PROGP + cb * PW : OOB;
  unsigned ov = obase + ((lane >> 4) & 1) * rt_stride + ((lane >> 5) * 32 + (lane & 15)) * 16;
  PINV(xv); PINV(progo); PINV(ov);
#pragma unroll
  for (int b = 0; b < 2; ++b)
#pragma unroll
    for (int i = 0; i < 5; ++i) dma2(X + (size_t)b * 40960 + i * 2048, xv, xl + b * 40960 + i * 2048);
  WAITV(10);
  bar();
  int buf = 0;
  v4u B[4], ob;
  f16v acc[2][2];
  float e0 = 0.f, e1 = 0.f, u0 = 0.f, u1 = 0.f; unsigned pgv = 0;
#pragma unroll
  for (int t = 0; t < 2; ++t)
#pragma unroll
    for (int r = 0; r < 16; ++r) acc[1][t][r] = 0.f;
#pragma unroll
  for (int i = 0; i < 3; ++i) B[i] = lr16(c.lb + lane * 16 + i * 1024);
  unsigned oprev = OOB;
  auto epi = [&](f16v (&a)[2], auto ST_, unsigned obyte) __attribute__((always_inline)) {
    constexpr int st = decltype(ST_)::v;
    if constexpr (MLP) {
      if constexpr (st >= 4 && st < 80) {
        constexpr int grp = (st - 4) / 19, o = (st - 4) % 19;
        constexpr int t = grp >> 1, u = grp & 1, j = o / 4, ph = o % 4, r = 8 * u + 2 * j;
        if constexpr (o < 16) {
          if constexpr (ph == 0) { e0 = a[t][r]; e1 = a[t][r + 1]; u0 = e0 * e0; u1 = e1 * e1; PINV(e0); PINV(e1); PINV(u0); PINV(u1); }
          if constexpr (ph == 1) {
            u0 = e0 * fmaf(u0, -0.10294324f, -2.3022082f); u1 = e1 * fmaf(u1, -0.10294324f, -2.3022082f); PINV(u0); PINV(u1);
          }
          if constexpr (ph == 2) { u0 = 1.f + ex2(u0); u1 = 1.f + ex2(u1); PINV(u0); PINV(u1); }
          if constexpr (ph == 3) { ob[j] = pk(e0 * rcp(u0), e1 * rcp(u1)); PINV(ob); }
        }
        if constexpr (o == 17) __builtin_amdgcn_raw_buffer_store_b128(ob, c.rs, ov + t * 1024 + u * 256, obyte, 16);
      }
    } else {
      if constexpr (st >= 4 && st < 28) {
        constexpr int grp = (st - 4) / 6, o = (st - 4) % 6, t = grp >> 1, u = grp & 1;
        if constexpr (o < 4) { ob[o] = pk(a[t][8 * u + 2 * o], a[t][8 * u + 2 * o + 1]); PINV(ob); }
        if constexpr (o == 5) __builtin_amdgcn_raw_buffer_store_b128(ob, c.rs, ov + t * 1024 + u * 256, obyte, 16);
      }
    }
  };
  for (int s2 = 0; s2 < NSEG; s2 += 2) {
    sfor<0, 2>([&](auto U_) __attribute__((always_inline)) {
      constexpr int cu = decltype(U_)::v;
      const int s = s2 + cu;
      const int b1 = buf == 2 ? 0 : buf + 1, b2 = buf == 0 ? 2 : buf - 1;
      unsigned xb = c.lb + buf * 40960 + lane * 16, xbn = c.lb + b1 * 40960 + lane * 16;
      const int sn = s + 2 < NSEG ? s + 2 : s;
      const u8* xs = X + (size_t)sn * 40960; const unsigned xd = xl + b2 * 40960;
      sfor<0, 80>([&](auto I_) __attribute__((always_inline)) {
        constexpr int i = decltype(I_)::v, ks = i >> 1, t = i & 1;
        if constexpr (ks == 0) acc[cu][t] = mfma32(A[t][0], B[0], bias[t]);
        else acc[cu][t] = mfma32(A[t][ks], B[ks & 3], acc[cu][t]);
        SB();
        if constexpr (t == 0) B[(ks + 3) & 3] = ks + 3 < 40 ? lr16(xb + (ks + 3) * 1024) : lr16(xbn + (ks + 3 - 40) * 1024);
        epi(acc[cu ^ 1], I_, oprev);
        if constexpr (i == 39) { pgv = MLP ? s - 1 : s; PINV(pgv); }
        if constexpr (i == 41) {
          if (MLP) WAITV(2); else WAITV(4);
          RAWBAR();
          if (MLP) st4(c.rs, progo, pgv);
        }
        if constexpr (i >= 43 && i < 73 && (i - 43) % 3 == 0) dma_sc1(xs + ((i - 43) / 3) * 1024, xv, xd + ((i - 43) / 3) * 1024);
        if constexpr (!MLP && i == 55) { WAITV(5); st4(c.rs, progo, pgv); }
        SB();
      });
      oprev = s * ostride;
      buf = b1;
    });
  }
  sfor<0, 80>([&](auto I_) __attribute__((always_inline)) { epi(acc[1], I_, oprev); });
  WAITV(0);
  st4(c.rs, progo, 2u * NSEG);
}

struct ChunkDma { unsigned v0[6]; unsigned v1; };
DEVI void chunk_init(ChunkDma& cd, int w, int lane) {
#pragma unroll
  for (int i = 0; i < 6; ++i) { unsigned x = (w * 6 + i) * 64 + lane; cd.v0[i] = (x / 24) * 12288 + (x % 24) * 16; }
  cd.v1 = (w * 48 + (lane >> 3)) * 1280 + (lane & 7) * 16;
}
DEVI void chunk_dma0(const ChunkDma& cd, const u8* src, unsigned slot, int w) {
#pragma unroll
  for (int i = 0; i < 6; ++i) dma(src, cd.v0[i], slot + (w * 6 + i) * 1024);
}
DEVI void chunk_dma1(const ChunkDma& cd, const u8* src, unsigned slot, int w) {
#pragma unroll
  for (int i = 0; i < 6; ++i) dma(src + i * 8 * 1280, cd.v1, slot + (w * 6 + i) * 1024);
}
DEVI v8u frag0(unsigned slot, int t, const Ctx& c) {
  const unsigned q = (c.lane >> 2) & 3, pp = c.lane & 3;
  const unsigned a = slot + (16 * (c.g >> 1) + 4 * (c.g & 1) + (q & 1) + 8 * (q >> 1)) * 384 + (64 * t + 16 * c.w + 4 * pp) * 2;
  return cat(cat(trd(a), trd(a + 2 * 384)), cat(trd(a + 32 * 384), trd(a + 34 * 384)));
}
DEVI v8u frag1(unsigned slot, int T, const Ctx& c) {
  const unsigned q = (c.lane >> 2) & 3, pp = c.lane & 3;
  const unsigned a = slot + (64 * T + 8 * c.g + q) * 128 + (16 * c.w + 4 * pp) * 2;
  return cat(cat(trd(a), trd(a + 4 * 128)), cat(trd(a + 32 * 128), trd(a + 36 * 128)));
}

DEVI void role_U(const Params& p, const Ctx& c, int h) {
  const int lane = c.lane, w = c.w, g = c.g, n = c.n;
  v8u B[10][3];
  {
    ChunkDma cd; chunk_init(cd, w, lane);
    const u8* Wo = p.wo + (size_t)h * 192 * 1280;
#pragma unroll
    for (int i = 0; i < 6; ++i) chunk_dma1(cd, Wo + i * 128, c.lb + i * 24576, w);
#pragma unroll
    for (int i = 0; i < 10; ++i) {
      if (i <= 4) WAITV(30); else WAITV(6 * (9 - i));
      bar();
#pragma unroll
      for (int T = 0; T < 3; ++T) B[i][T] = frag1(c.lb + (i % 6) * 24576, T, c);
      if (i + 6 < 10) { bar(); chunk_dma1(cd, Wo + (i + 6) * 128, c.lb + (i % 6) * 24576, w); }
    }
  }
#pragma unroll
  for (int i = 0; i < 10; ++i)
#pragma unroll
    for (int T = 0; T < 3; ++T) PINA(B[i][T]);
  bar();
  EV(1);
  bool dead = false; int have = 0;
  const u8* Vs = p.ws + O_VX + h * 12288 + w * 3072;
  const unsigned vl = c.lb + w * 3072;
  const unsigned progV = C_PROGP + (192 + 3 * h) * PW, prog = C_PROGU + (w * 32 + h) * PW;
  const unsigned al = c.lb + lane * 16;
  wait_prog(c.rs, progV, 3, 0, have, dead, lane);
  dma3_sc1(Vs, lane * 16, vl);
  WAITV(0);
  bar();
  v4u A[2][2][6];
#pragma unroll
  for (int T = 0; T < 6; ++T) { A[0][0][T] = lr16(al + T * 1024); A[0][1][T] = lr16(al + 6144 + T * 1024); }
  f4 a[2][2] = {{{0, 0, 0, 0}, {0, 0, 0, 0}}, {{0, 0, 0, 0}, {0, 0, 0, 0}}};
  unsigned oprev = OOB, ov = O_U + (w * 32 + h) * 1024 + lane * 16, pgo = lane == 0 ? prog : OOB, pvo = progV + (lane < 3 ? lane : 0) * PW, pgv = 0;
  int pv = 0;
  PINV(ov); PINV(pgo); PINV(pvo);
  v4u ob;
  int buf = 0, staged = 1; bool next_ok = false;
  unsigned pl = c.lb + 40960 + lane * 4; PINV(pl);
  for (int s2 = 0; s2 < NSEG; s2 += 2) {
    sfor<0, 2>([&](auto U_) __attribute__((always_inline)) {
      constexpr int u = decltype(U_)::v;
      const int s = s2 + u;
      const int b1 = buf == 2 ? 0 : buf + 1;
      const unsigned ob_s = s * (40 * 32768);
      unsigned an = al + b1 * 12288;
      sfor<0, 120>([&](auto I_) __attribute__((always_inline)) {
        constexpr int q = decltype(I_)::v, i = q / 12, e = q % 12, T = e >> 1, tt = e & 1;
        const v4u bb = (T & 1) ? hi4(B[i][T >> 1]) : lo4(B[i][T >> 1]);
        if constexpr (T == 0) a[i & 1][tt] = mfma(A[u][tt][T], bb, (f4){0, 0, 0, 0}); else a[i & 1][tt] = mfma(A[u][tt][T], bb, a[i & 1][tt]);
        SB();
        if constexpr (e >= 2 && e < 10 && (e & 1) == 0) { constexpr int j = (e - 2) / 2; const f4 x = a[(i & 1) ^ 1][j >> 1]; ob[j] = pk(x[2 * (j & 1)], x[2 * (j & 1) + 1]); PINV(ob); }
        if constexpr (e == 10) __builtin_amdgcn_raw_buffer_store_b128(ob, c.rs, ov, i == 0 ? oprev : ob_s + (i - 1) * 131072, 16);
        if constexpr (q == 1) { pgv = s; PINV(pgv); asm volatile("buffer_load_dword %0, %1, %2, 0 offen sc1" : "=v"(pv) : "v"(pvo), "s"(c.rs) : "memory"); }
        if constexpr (q == 59) {
          asm volatile("s_waitcnt vmcnt(4)" : "+v"(pv)::"memory");
          if (w == 0) lw4(pl, (unsigned)pv);
          bar();
          next_ok = staged > s + 1;
          st4(c.rs, pgo, pgv);
          pv = (int)*(LAS unsigned*)(uintptr_t)pl;
          for (int k = 0; k < 2; ++k)
            if (staged <= s + 2 && staged < NSEG && __builtin_amdgcn_ballot_w64(pv <= staged) == 0) {
              dma3_sc1(Vs + (size_t)staged * (32 * 12288), lane * 16, vl + (staged % 3) * 12288); ++staged;
            }
        }
        if constexpr (q >= 73 && q < 97 && (q & 1)) { constexpr int z = (q - 73) / 2; A[u ^ 1][z / 6][z % 6] = lr16(an + z * 1024); }
        SB();
      });
      if (!next_ok && s + 1 < NSEG) {
        constexpr int la = 1;
        __builtin_amdgcn_raw_buffer_store_b128((v4u){pk(a[la][0][0], a[la][0][1]), pk(a[la][0][2], a[la][0][3]), pk(a[la][1][0], a[la][1][1]), pk(a[la][1][2], a[la][1][3])}, c.rs, ov, ob_s + 9 * 131072, 16);
        WAITV(0);
        st4(c.rs, pgo, (unsigned)(s + 1));
        if (staged <= s + 1) {
          have = 0; wait_prog(c.rs, progV, 3, s + 1, have, dead, lane);
          dma3_sc1(Vs + (size_t)(s + 1) * (32 * 12288), lane * 16, vl + b1 * 12288); ++staged;
          WAITV(0);
        }
        RAWBAR();
#pragma unroll
        for (int z = 0; z < 12; ++z) A[u ^ 1][z / 6][z % 6] = lr16(an + z * 1024);
      }
      oprev = ob_s + 9 * 131072;
      buf = b1;
    });
  }
  __builtin_amdgcn_raw_buffer_store_b128((v4u){pk(a[1][0][0], a[1][0][1]), pk(a[1][0][2], a[1][0][3]), pk(a[1][1][0], a[1][1][1]), pk(a[1][1][2], a[1][1][3])}, c.rs, ov, oprev, 16);
  WAITV(0);
  st4(c.rs, pgo, 2u * NSEG);
}

DEVI void role_G(const Params& p, const Ctx& c, int j, int par) {
  const int lane = c.lane, w = c.w, g = c.g, n = c.n;
  v4u A[5][12];
  {
    const unsigned st = c.lb + w * 30720;
    unsigned voff[10];
#pragma unroll
    for (int i = 0; i < 10; ++i) { unsigned x = i * 64 + lane; voff[i] = (x / 10) * 1280 + (x % 10) * 16; }
    const u8* W = p.w2 + (size_t)w * 384 * 1280 + j * 160;
    const unsigned q = (lane >> 2) & 3, pp = lane & 3;
    const unsigned ra = st + (8 * g + q) * 160 + pp * 8;
#pragma unroll
    for (int ch = 0; ch < 3; ++ch)
#pragma unroll
      for (int i = 0; i < 10; ++i) dma(W + (size_t)ch * 64 * 1280, voff[i], st + ch * 10240 + i * 1024);
#pragma unroll
    for (int ch = 0; ch < 6; ++ch) {
      if (ch <= 3) WAITV(20); else if (ch == 4) WAITV(10); else WAITV(0);
#pragma unroll
      for (int kk = 0; kk < 2; ++kk)
#pragma unroll
        for (int t = 0; t < 5; ++t) {
          const unsigned a = ra + (ch % 3) * 10240 + kk * 5120 + t * 32;
          A[t][ch * 2 + kk] = cat(trd(a), trd(a + 640));
        }
      if (ch + 3 < 6) {
        asm volatile("s_waitcnt lgkmcnt(0)" ::: "memory");
#pragma unroll
        for (int i = 0; i < 10; ++i) dma(W + (size_t)(ch + 3) * 64 * 1280, voff[i], st + (ch % 3) * 10240 + i * 1024);
      }
    }
  }
  float bias[3][4];
#pragma unroll
  for (int k = 0; k < 3; ++k) {
    int tl = w + 4 * k; if (tl > 9) tl = 9;
    v2u bv = *(const v2u*)(p.b2 + (j * 80 + (tl >> 1) * 16 + 4 * g) * 2);
    bias[k][0] = blo(bv.x); bias[k][1] = bhi(bv.x); bias[k][2] = blo(bv.y); bias[k][3] = bhi(bv.y);
  }
  bar();
#pragma unroll
  for (int ks = 0; ks < 10; ++ks)
#pragma unroll
    for (int t = 0; t < 5; ++t) PINA(A[t][ks]);
  bool dead = false; int have = 0;
  const unsigned hoff = O_H1 + w * 12288 + lane * 16;
  const unsigned prog = C_PROGG + ((par * 8 + j) * 4 + w) * PW, progH = C_PROGP + (288 + 6 * w) * PW;
  v4u B[2][12];
  wait_prog(c.rs, progH, 6, par, have, dead, lane);
#pragma unroll
  for (int rt = 0; rt < 2; ++rt)
#pragma unroll
    for (int ks = 0; ks < 12; ++ks) B[rt][ks] = ld16(c.rs, hoff + par * 98304 + rt * 49152 + ks * 1024);
  unsigned pvo = progH + (lane < 6 ? lane : 0) * PW, pgo = lane == 0 ? prog : OOB, pgv = 0, ra = c.lb + lane * 16, wa = c.lb + w * 10240 + lane * 16, gv8 = lane * 8;
  PINV(pvo); PINV(pgo); PINV(ra); PINV(wa); PINV(gv8);
  int pv = 0;
  f4 r4[4], z4; float e4[4]; v2u o2;
  auto epi = [&](auto K_, auto E_, int sp, unsigned rb) __attribute__((always_inline)) {
    constexpr int k = decltype(K_)::v, e = decltype(E_)::v;
    const int tl = min(w + 4 * k, 9);
    if constexpr (e < 4) r4[e] = lrf4(ra + rb + tl * 1024 + e * 10240);
    if constexpr (e >= 8 && e < 12) { z4[e - 8] = (r4[0][e - 8] + r4[1][e - 8]) + (r4[2][e - 8] + r4[3][e - 8]); PINV(z4); }
    if constexpr (e >= 12 && e < 16) { z4[e - 12] = rbf(z4[e - 12]) + bias[k][e - 12]; PINV(z4); }
    if constexpr (e >= 16 && e < 20) { z4[e - 16] = rbf(z4[e - 16]); e4[e - 16] = -1.4426950408889634f * z4[e - 16]; PINV(z4); PINV(e4[e - 16]); }
    if constexpr (e >= 20 && e < 24) { e4[e - 20] = 1.f + ex2(e4[e - 20]); PINV(e4[e - 20]); }
    if constexpr (e >= 24 && e < 28) { e4[e - 24] = z4[e - 24] * rcp(e4[e - 24]); PINV(e4[e - 24]); }
    if constexpr (e == 28) { o2 = (v2u){pk(e4[0], e4[1]), pk(e4[2], e4[3])}; PINV(o2); }
    if constexpr (e == 30) __builtin_amdgcn_raw_buffer_store_b64(o2, c.rs, gv8, (w + 4 * k < 10 && sp >= 0) ? O_G + ((sp * 40 + j * 5 + (tl >> 1)) * 2 + (tl & 1)) * 512 : OOB, 16);
  };
  for (int s = par; s < NSEG; s += 2) {
    if (s + 2 < NSEG && __builtin_amdgcn_ballot_w64(pv <= s + 2) != 0) { have = 0; wait_prog(c.rs, progH, 6, s + 2, have, dead, lane); }
    unsigned on = s + 2 < NSEG ? hoff + (s + 2) * 98304 : OOB;
    PINV(on);
    const unsigned rb = ((s >> 1) & 1) * 40960, rp = 40960 - rb;
    f4 acc[2][5];
    sfor<0, 120>([&](auto I_) __attribute__((always_inline)) {
      constexpr int i = decltype(I_)::v, rt = i / 60, ks = (i % 60) / 5, t = i % 5;
      if constexpr (ks == 0) acc[rt][t] = mfma(A[t][0], B[rt][0], (f4){0, 0, 0, 0}); else acc[rt][t] = mfma(A[t][ks], B[rt][ks], acc[rt][t]);
      SB();
      if constexpr (t == 4) B[rt][ks] = __builtin_amdgcn_raw_buffer_load_b128(c.rs, on + (ks & 3) * 1024, rt * 49152 + (ks >> 2) * 4096, 16);
      if constexpr (i < 108) epi(IC<i / 36>{}, IC<i % 36>{}, s - 2, rp);
      if constexpr (i >= 66 && i < 76 && (i & 1) == 0) lwf4(wa + rb + (i - 66) * 1024, acc[0][(i - 66) / 2]);
      if constexpr (i == 112) pv = (int)ld4(c.rs, pvo);
      SB();
    });
#pragma unroll
    for (int t = 0; t < 5; ++t) lwf4(wa + rb + (t * 2 + 1) * 1024, acc[1][t]);
    WAITV(5);
    st4(c.rs, pgo, (unsigned)s);
    bar();
  }
  sfor<0, 108>([&](auto I_) __attribute__((always_inline)) { constexpr int i = decltype(I_)::v; epi(IC<i / 36>{}, IC<i % 36>{}, NSEG - 2 + par, 40960); });
  WAITV(0);
  st4(c.rs, pgo, 2u * NSEG);
}

constexpr unsigned SV_MEMI = 122880, SV_I2 = SV_MEMI, SV_SMX = SV_MEMI + 4096, SV_I1 = SV_MEMI + 10240, SV_QX = SV_I1 + 3072, SV_KX = SV_QX + 12288;
constexpr unsigned SV_FLG = LDS_BYTES - 16, SV_SMP = LDS_BYTES - 80;
constexpr unsigned SV_SCR = SV_I1;
static_assert(SV_KX + 12288 <= LDS_BYTES, "S LDS map");
DEVI constexpr int res_slot(int ci) { return ci == 0 ? 0 : ci == 4 ? 1 : ci == 8 ? 2 : ci == 13 ? 3 : ci == 17 ? 4 : -1; }
DEVI constexpr int load_order(int i) {
  return i < 3 ? i + 1 : i < 6 ? i + 2 : i < 10 ? i + 3 : i < 13 ? i + 4 : i < 15 ? i + 5 : i == 15 ? 0 : i == 16 ? 4 : i == 17 ? 8 : i == 18 ? 13 : 17;
}
DEVI constexpr int reg_idx(int ci) { return ci < 4 ? ci - 1 : ci < 8 ? ci - 2 : ci < 13 ? ci - 3 : ci < 17 ? ci - 4 : ci - 5; }

template <bool ISV>
DEVI void sv_load_weights(const Params& p, const Ctx& c, int h, v8u (&W)[15][3]) {
  ChunkDma cd; chunk_init(cd, c.w, c.lane);
  const u8* W1 = (ISV ? p.wv : p.wq) + h * 384;
  const u8* W2 = ISV ? p.wo + (size_t)h * 192 * 1280 : p.wk + h * 384;
  auto issue = [&](int i) {
    const int ci = load_order(i); const unsigned slot = c.lb + ((i + 3) % 6) * 24576;
    if (ci < 10) chunk_dma0(cd, W1 + (size_t)ci * 64 * 12288, slot, c.w);
    else if (ISV) chunk_dma1(cd, W2 + (ci - 10) * 128, slot, c.w);
    else chunk_dma0(cd, W2 + (size_t)(ci - 10) * 64 * 12288, slot, c.w);
  };
#pragma unroll
  for (int i = 0; i < 6; ++i) issue(i);
#pragma unroll
  for (int i = 0; i < 20; ++i) {
    if (i <= 14) WAITV(30); else WAITV(6 * (19 - i));
    bar();
    const int ci = load_order(i); const unsigned slot = c.lb + ((i + 3) % 6) * 24576;
    v8u f[3];
#pragma unroll
    for (int t = 0; t < 3; ++t) f[t] = (ISV && ci >= 10) ? frag1(slot, t, c) : frag0(slot, t, c);
    bar();
    if (i < 15) {
#pragma unroll
      for (int t = 0; t < 3; ++t) { W[i][t] = f[t]; if (i * 3 + t < 32) PINA(W[i][t]); else PINV(W[i][t]); }
      if (i + 6 < 20) issue(i + 6);
    } else {
#pragma unroll
      for (int t = 0; t < 3; ++t) { const unsigned a = slot + (c.w * 3 + t) * 2048 + c.lane * 16; lw16(a, lo4(f[t])); lw16(a + 1024, hi4(f[t])); }
    }
  }
  bar();
}
struct ResW { unsigned lo, hi; };
DEVI v4u res_h(const ResW& r, int ci, int t, int hf) {
  const int sl = res_slot(ci);
  return sl < 2 ? lr16(r.lo + sl * 24576 + t * 2048 + hf * 1024) : lr16(r.hi + (sl - 2) * 24576 + t * 2048 + hf * 1024);
}
DEVI v8u res_w(const ResW& r, int ci, int t) { return cat(res_h(r, ci, t, 0), res_h(r, ci, t, 1)); }
DEVI void sv_gather(const Ctx& c, unsigned gbase, unsigned cls, unsigned l0, unsigned l2, bool& dead, unsigned plan, unsigned planv, int s);
DEVI void sv_gather_(const Ctx& c, unsigned o0, unsigned o1, unsigned o2, unsigned cnt, unsigned l0, unsigned l2, bool& dead) {
  v4u a, b, d; unsigned m;
  asm volatile(
      "s_barrier\n\t"
      "1:\n\t"
      "buffer_load_dwordx4 %[a], %[o0], %[rs], 0 offen sc1\n\t"
      "buffer_load_dwordx4 %[b], %[o1], %[rs], 0 offen sc1\n\t"
      "buffer_load_dwordx4 %[d], %[o2], %[rs], 0 offen sc1\n\t"
      "s_sub_u32 %[cnt], %[cnt], 1\n\t"
      "s_waitcnt vmcnt(2)\n\t"
      "ds_write_b128 %[l0], %[a]\n\t"
      "v_max3_u32 %[m], v240, v241, v242\n\t"
      "s_waitcnt vmcnt(1)\n\t"
      "ds_write_b128 %[l0], %[b] offset:4096\n\t"
      "v_max3_u32 %[m], %[m], v243, v244\n\t"
      "v_max3_u32 %[m], %[m], v245, v246\n\t"
      "s_waitcnt vmcnt(0)\n\t"
      "ds_write_b128 %[l2], %[d]\n\t"
      "v_max3_u32 %[m], %[m], v247, v248\n\t"
      "v_max3_u32 %[m], %[m], v249, v250\n\t"
      "v_max_u32 %[m], %[m], v251\n\t"
      "v_cmp_eq_u32 vcc, -1, %[m]\n\t"
      "s_cbranch_vccz 2f\n\t"
      "s_cmp_lg_u32 %[cnt], 0\n\t"
      "s_waitcnt lgkmcnt(0)\n\t"
      "s_cbranch_scc1 1b\n\t"
      "2:\n\t"
      "s_waitcnt lgkmcnt(0)\n\t"
      "s_barrier"
      : [a] "=&{v[240:243]}"(a), [b] "=&{v[244:247]}"(b), [d] "=&{v[248:251]}"(d), [m] "=&v"(m), [cnt] "+s"(cnt)
      : [o0] "v"(o0), [o1] "v"(o1), [o2] "v"(o2), [rs] "s"(c.rs), [l0] "v"(l0), [l2] "v"(l2)
      : "vcc", "scc", "memory");
  if (cnt == 0) dead = true;
}
DEVI unsigned now32() { return (unsigned)__builtin_amdgcn_s_memrealtime(); }
template <int CTRL> DEVI int dppi(int a) { return __builtin_amdgcn_update_dpp(a, a, CTRL, 0xF, 0xF, false); }
DEVI void wait_time(unsigned t) { int e; do { e = (int)(t - now32()); } while (e > 0 && e < 1000); }
typedef f2 f2_t;
DEVI unsigned max_t(unsigned a, unsigned b) { const int d = (int)(a - b); return (d > 0 && d < 1000) ? a : b; }
DEVI void hint_put(const Ctx& c, unsigned off, unsigned t, int tag) {
  unsigned z; asm volatile("v_mov_b32 %0, 0" : "=v"(z));
  __builtin_amdgcn_raw_buffer_store_b32((t & 0xFFFFFFu) | ((unsigned)tag << 24), c.rs, z, off, 16);
}
DEVI unsigned plan_load(const Ctx& c, unsigned off) {
  unsigned z; asm volatile("v_mov_b32 %0, 0" : "=v"(z));
  return __builtin_amdgcn_raw_buffer_load_b32(c.rs, z, off, 16);
}
DEVI void plan_wait(const Ctx& c, unsigned off, unsigned vv, int tag, int extra = 0) {
  unsigned v = __builtin_amdgcn_readfirstlane(vv);
  for (int it = 0; (v >> 24) != (unsigned)tag; ++it) {
    if (tag < 2 || it >= 6) return;
    __builtin_amdgcn_s_sleep(3);
    FENCE();
    v = __builtin_amdgcn_readfirstlane(plan_load(c, off));
  }
  const unsigned t0 = now32();
  wait_time(t0 + (unsigned)(((int)((v - t0) << 8) >> 8) + extra));
}
DEVI int window_wait(const Ctx& c, unsigned plan, int& last) {
  if (last >= NSEG - 1) return 0;
  for (int it = 0; it < 24; ++it) {
    FENCE();
    const unsigned v = __builtin_amdgcn_readfirstlane(plan_load(c, plan));
    const int tag = v >> 24;
    if (tag > last && tag <= NSEG) {
      const unsigned t0 = now32();
      const int d = ((int)((v - t0) << 8) >> 8) + c.kw;
      last = tag;
      if (d > -60) { wait_time(t0 + d); return tag; }
    }
    __builtin_amdgcn_s_sleep(6);
  }
  return 0;
}
DEVI void sv_gather(const Ctx& c, unsigned gbase, unsigned cls, unsigned l0, unsigned l2, bool& dead, unsigned plan, unsigned planv, int s) {
  const int w = c.w, f2 = w < 2 ? 8 + w : w;
  unsigned o0 = amap(gbase + w * 1024, cls) + c.lane * 16, o1 = amap(gbase + (4 + w) * 1024, cls) + c.lane * 16, o2 = w < 2 ? amap(gbase + f2 * 1024, cls) + c.lane * 16 : OOB;
  unsigned cnt = dead ? 1u : SPIN_CAP;
  PINV(o0); PINV(o1); PINV(o2); asm volatile("" : "+s"(cnt));
  WAITV(0);
  if (w == 0) plan_wait(c, plan, planv, s, c.kv);
  sv_gather_(c, o0, o1, o2, cnt, l0, l2, dead);
}
DEVI void s_gather(const Ctx& c, unsigned gbase, unsigned cls, unsigned ma, bool& dead, unsigned plan, unsigned planv, int s, unsigned long long* st_) {
  const unsigned hw = c.w & 1;
  unsigned o0 = amap(gbase + hw * 4096, cls) + c.lane * 16, o1 = amap(gbase + 8192 + hw * 1024, cls) + c.lane * 16;
  unsigned l0 = ma + hw * 4096, l1 = ma + 8192 + hw * 1024;
  unsigned cnt = dead ? 1u : SPIN_CAP;
  PINV(o0); PINV(o1); PINV(l0); PINV(l1); asm volatile("" : "+s"(cnt));
  WAITV(0);
  lw4(c.lb + SV_FLG + hw * 4, (unsigned)s);
  ST(3);
  plan_wait(c, plan, planv, s);
  ST(0);
  v4u a, b, d, e, f; unsigned m;
  asm volatile(
      "1:\n\t"
      "buffer_load_dwordx4 %[a], %[o0], %[rs], 0 offen sc1\n\t"
      "buffer_load_dwordx4 %[b], %[o0], %[rs], 0 offen offset:1024 sc1\n\t"
      "buffer_load_dwordx4 %[d], %[o0], %[rs], 0 offen offset:2048 sc1\n\t"
      "buffer_load_dwordx4 %[e], %[o0], %[rs], 0 offen offset:3072 sc1\n\t"
      "buffer_load_dwordx4 %[f], %[o1], %[rs], 0 offen sc1\n\t"
      "s_sub_u32 %[cnt], %[cnt], 1\n\t"
      "s_waitcnt vmcnt(4)\n\t"
      "ds_write_b128 %[l0], %[a]\n\t"
      "v_max3_u32 %[m], v232, v233, v234\n\t"
      "s_waitcnt vmcnt(3)\n\t"
      "ds_write_b128 %[l0], %[b] offset:1024\n\t"
      "v_max3_u32 %[m], %[m], v235, v236\n\t"
      "v_max3_u32 %[m], %[m], v237, v238\n\t"
      "s_waitcnt vmcnt(2)\n\t"
      "ds_write_b128 %[l0], %[d] offset:2048\n\t"
      "v_max3_u32 %[m], %[m], v239, v240\n\t"
      "v_max3_u32 %[m], %[m], v241, v242\n\t"
      "s_waitcnt vmcnt(1)\n\t"
      "ds_write_b128 %[l0], %[e] offset:3072\n\t"
      "v_max3_u32 %[m], %[m], v243, v244\n\t"
      "v_max3_u32 %[m], %[m], v245, v246\n\t"
      "s_waitcnt vmcnt(0)\n\t"
      "ds_write_b128 %[l1], %[f]\n\t"
      "v_max3_u32 %[m], %[m], v247, v248\n\t"
      "v_max3_u32 %[m], %[m], v249, v250\n\t"
      "v_max_u32 %[m], %[m], v251\n\t"
      "v_cmp_eq_u32 vcc, -1, %[m]\n\t"
      "s_cbranch_vccz 2f\n\t"
      "s_cmp_lg_u32 %[cnt], 0\n\t"
      "s_waitcnt lgkmcnt(0)\n\t"
      "s_cbranch_scc1 1b\n\t"
      "2:\n\t"
      "s_waitcnt lgkmcnt(0)"
      : [a] "=&{v[232:235]}"(a), [b] "=&{v[236:239]}"(b), [d] "=&{v[240:243]}"(d), [e] "=&{v[244:247]}"(e), [f] "=&{v[248:251]}"(f),
        [m] "=&v"(m), [cnt] "+s"(cnt)
      : [o0] "v"(o0), [o1] "v"(o1), [rs] "s"(c.rs), [l0] "v"(l0), [l1] "v"(l1)
      : "vcc", "scc", "memory");
  if (cnt == 0) dead = true;
}
DEVI unsigned fold(f4 a) { return pk(a[0] + a[2], a[1] + a[3]); }
DEVI v4u tr2(unsigned a, unsigned d) { return cat(trd(a), trd(a + d)); }
DEVI unsigned img_wr(const Ctx& c) { const unsigned e = 16 * c.w + c.n; return ((e >> 2) * 2 + (e & 1)) * 32 + c.g * 8 + ((e >> 1) & 1) * 4; }
DEVI unsigned img_rd(const Ctx& c) { return (8 * c.g + ((c.lane >> 2) & 3)) * 32 + (c.lane & 3) * 8; }
#define ZERO4 ((f4){0, 0, 0, 0})
DEVI f4 zero4() { f2 a, b; asm volatile("v_mov_b64 %0, 0\n\tv_mov_b64 %1, 0" : "=v"(a), "=v"(b)); f4 r = __builtin_shufflevector(a, b, 0, 1, 2, 3); PINV(r); return r; }
DEVI unsigned sent1() { unsigned a; asm volatile("v_mov_b32 %0, -1" : "=v"(a)); return a; }
DEVI v2u sent2() { v2u a; asm volatile("v_mov_b64 %0, -1" : "=v"(a)); return a; }
DEVI v4u sent4() { return cat(sent2(), sent2()); }
DEVI void mfma_v(f4& acc, v4u a, v4u b) { asm volatile("v_mfma_f32_16x16x32_bf16 %0, %1, %2, %0" : "+v"(acc) : "v"(a), "v"(b)); }
DEVI void mfma_v_last(f4& acc, v4u a, v4u b) { asm volatile("v_mfma_f32_16x16x32_bf16 %0, %1, %2, %0\n\ts_nop 7" : "+v"(acc) : "v"(a), "v"(b)); }
DEVI float rowmax8(float x) {
  float t;
  asm("s_nop 1\n\tv_max_f32_dpp %0, %1, %1 quad_perm:[1,0,3,2] row_mask:0xf bank_mask:0xf\n\t"
      "s_nop 1\n\tv_max_f32_dpp %0, %0, %0 quad_perm:[2,3,0,1] row_mask:0xf bank_mask:0xf\n\t"
      "s_nop 1\n\tv_max_f32_dpp %0, %0, %0 row_half_mirror row_mask:0xf bank_mask:0xf" : "=&v"(t) : "v"(x));
  return t;
}
DEVI float rowsum8(float x) {
  float t;
  asm("s_nop 1\n\tv_add_f32_dpp %0, %1, %1 quad_perm:[1,0,3,2] row_mask:0xf bank_mask:0xf\n\t"
      "s_nop 1\n\tv_add_f32_dpp %0, %0, %0 quad_perm:[2,3,0,1] row_mask:0xf bank_mask:0xf\n\t"
      "s_nop 1\n\tv_add_f32_dpp %0, %0, %0 row_half_mirror row_mask:0xf bank_mask:0xf" : "=&v"(t) : "v"(x));
  return t;
}
DEVI float mfma_fold(f4 acc, v4u a, v4u b) {
  float t;
  asm volatile("v_mfma_f32_16x16x32_bf16 v[252:255], %1, %2, %3\n\ts_nop 7\n\t"
               "v_add_f32_dpp %0, v254, v252 row_ror:8 row_mask:0xf bank_mask:0x3\n\t"
               "v_add_f32_dpp %0, v253, v255 row_ror:8 row_mask:0xf bank_mask:0xc"
               : "=&v"(t) : "v"(a), "v"(b), "v"(acc) : "v252", "v253", "v254", "v255");
  return t;
}
DEVI unsigned pkpos(float a, float b) { unsigned r; asm("v_cvt_pk_bf16_f32 %0, |%1|, |%2|" : "=v"(r) : "v"(a), "v"(b)); return r; }
DEVI float vmax(float a, float b) { return __builtin_elementwise_maximum(a, b); }
template <int CTRL> DEVI float dppf(float a) { return __int_as_float(__builtin_amdgcn_mov_dpp(__float_as_int(a), CTRL, 0xF, 0xF, true)); }

DEVI constexpr int vks(int j) { return j == 0 ? 1 : j == 1 ? 2 : j == 2 ? 0 : j == 3 ? 3 : j == 4 ? 5 : j == 5 ? 4 : j == 6 ? 6 : j == 7 ? 7 : j == 8 ? 9 : 8; }
DEVI void role_V(const Params& p, const Ctx& c, int h, unsigned cls0, unsigned cls1) {
  const int lane = c.lane, w = c.w, g = c.g, n = c.n;
  v8u W[15][3];
  sv_load_weights<true>(p, c, h, W);
  EV(1);
  const int idx = (lane & 2) ? 0xEEEE : 0x4444;
  ResW rw; rw.lo = c.lb + w * 6144 + lane * 16; rw.hi = rw.lo + 49152; PINV(rw.lo); PINV(rw.hi);
  unsigned iw = c.lb + SV_I1 + img_wr(c), ir = c.lb + SV_I1 + img_rd(c), ma = c.lb + SV_MEMI + lane * 16;
  unsigned l0 = ma + w * 1024, l2 = w < 2 ? ma + (8 + w) * 1024 : c.lb + SV_SCR + lane * 16;
  PINV(iw); PINV(ir); PINV(ma); PINV(l0); PINV(l2);
  const unsigned yo = w * 16384 + h * 256 + lane * 4;
  bool dead = false;
  int slot = 0;
  const unsigned plan = amap(L_CLK, cls0) + (40 + c.x * 2) * 128;
  ST_DECL
  for (int s = 0; s < NSEG; ++s) {
    v4u T[3][2];
#pragma unroll
    for (int t = 0; t < 3; ++t) { T[t][0] = res_h(rw, 4 * t, 0, 0); T[t][1] = res_h(rw, 4 * t, 0, 1); }
    f4 acc[3], y[10];
#pragma unroll
    for (int t = 0; t < 3; ++t) acc[t] = zero4();
#pragma unroll
    for (int t = 0; t < 10; ++t) y[t] = zero4();
    const unsigned yb = slot < 2 ? amap(L_Y01 + slot * L_Y_SLOT, cls0) : WIN + cls1;
    unsigned ya = yo; PINV(ya);
    SB();
    sv_gather(c, L_MEM + slot * L_MEM_SLOT + c.x * L_MEM_REP, cls0, l0, l2, dead, plan, plan_load(c, w == 0 ? plan : OOB), s);
    ST(1);
    CT_DECL CT(0);
    v4u A[10], A2[3];
    float y0 = 0.f, y1 = 0.f; unsigned yv = 0;
    auto ldH = [&](int t, int ci, int tile, int hf) __attribute__((always_inline)) { T[t][hf] = res_h(rw, ci, tile, hf); };
    auto fold1 = [&](f4 a, int st) __attribute__((always_inline)) {
      if (st == 0) { y0 = a[0] + a[2]; PINV(y0); }
      if (st == 1) { y1 = a[1] + a[3]; PINV(y1); }
      if (st == 2) { yv = pk(y0, y1); PINV(yv); }
    };
#pragma unroll
    for (int j = 0; j < 5; ++j) { A[vks(j)] = lr16(ma + vks(j) * 1024); SB(); }
    sfor<0, 60>([&](auto I_) __attribute__((always_inline)) {
      constexpr int i = decltype(I_)::v;
      if constexpr (i < 30) {
        constexpr int t = i / 10, ks = vks(i % 10);
        if constexpr (res_slot(ks) >= 0) acc[t] = smf(A[ks], cat(T[ks / 4][0], T[ks / 4][1]), acc[t], idx); else acc[t] = smf(A[ks], W[reg_idx(ks)][t], acc[t], idx);
      } else {
        constexpr int Tk = i < 40 ? 0 : i < 43 ? 1 : i >= 57 ? 2 : (i & 1) ? 2 : 1;
        constexpr int tl = i < 40 ? i - 30 : i < 43 ? i - 40 : i >= 57 ? i - 50 : (i & 1) ? (i - 43) / 2 : 3 + (i - 44) / 2;
        constexpr int ti = tl == 3 ? (Tk == 1 ? 2 : 0) : (Tk == 2 ? 2 : 1);
        if constexpr (res_slot(10 + tl) >= 0) y[tl] = smf(A2[Tk], cat(T[ti][0], T[ti][1]), y[tl], idx); else y[tl] = smf(A2[Tk], W[reg_idx(10 + tl)][Tk], y[tl], idx);
      }
      SB();
      if constexpr (i < 5) A[vks(5 + i)] = lr16(ma + vks(5 + i) * 1024);
      if constexpr (i < 20 && (i % 10 == 5 || i % 10 == 6)) ldH(0, 0, i / 10 + 1, (i % 10) & 1 ^ 1);
      if constexpr (i < 20 && (i % 10 == 7 || i % 10 == 8)) ldH(1, 4, i / 10 + 1, (i % 10) & 1 ^ 1);
      if constexpr (i >= 10 && i < 30 && (i % 10 == 0 || i % 10 == 1)) ldH(2, 8, i / 10, i & 1);
      if constexpr (i >= 13 && i < 16) fold1(acc[0], i - 13);
      if constexpr (i == 19) lw4(iw, yv);
      if constexpr (i == 22) { asm volatile("s_waitcnt lgkmcnt(2)\n\ts_barrier" ::: "memory"); A2[0] = tr2(ir, 128); }
      if constexpr (i == 23 || i == 24) ldH(0, 13, 0, i & 1 ^ 1);
      if constexpr (i >= 23 && i < 26) fold1(acc[1], i - 23);
      if constexpr (i == 26 || i == 27) ldH(1, 17, 0, i & 1);
      if constexpr (i == 28) lw4(iw + 1024, yv);
      if constexpr (i == 30 || i == 31) ldH(2, 13, 1, i & 1);
      if constexpr (i == 32) { fold1(acc[2], 0); fold1(acc[2], 1); }
      if constexpr (i == 33) fold1(acc[2], 2);
      if constexpr (i == 34) { asm volatile("s_waitcnt lgkmcnt(0)\n\ts_barrier" ::: "memory"); lw4(iw + 2048, yv); }
      if constexpr (i == 35) A2[1] = tr2(ir + 1024, 128);
      if constexpr (i == 38) { asm volatile("s_waitcnt lgkmcnt(2)\n\ts_barrier" ::: "memory"); A2[2] = tr2(ir + 2048, 128); }
      if constexpr (i == 39 || i == 40) ldH(0, 13, 2, i & 1 ^ 1);
      if constexpr (i == 41 || i == 42) ldH(1, 17, 1, i & 1 ^ 1);
      if constexpr (i == 45 || i == 46) ldH(2, 17, 2, i & 1 ^ 1);
      if constexpr (i >= 48 && (i & 1) == 0) { __builtin_amdgcn_raw_buffer_store_b32(yv, c.rs, ya, yb + ((i - 48) / 2) * 65536, 16); SB(); }
      if constexpr (i >= 46 && (i & 1) == 0) { fold1(y[(i - 46) / 2], 0); asm volatile("" ::"v"(yv)); }
      if constexpr (i >= 47 && (i & 1) == 1) { fold1(y[(i - 47) / 2], 1); fold1(y[(i - 47) / 2], 2); }
      SB();
    });
    {
      unsigned u7 = fold(y[7]); PINV(u7);
      __builtin_amdgcn_raw_buffer_store_b32(yv, c.rs, ya, yb + 6 * 65536, 16);
      SB();
      unsigned u8_ = fold(y[8]), u9 = fold(y[9]); PINV(u8_); PINV(u9);
      __builtin_amdgcn_raw_buffer_store_b32(u7, c.rs, ya, yb + 7 * 65536, 16);
      __builtin_amdgcn_raw_buffer_store_b32(u8_, c.rs, ya, yb + 8 * 65536, 16);
      __builtin_amdgcn_raw_buffer_store_b32(u9, c.rs, ya, yb + 9 * 65536, 16);
    }
    SB();
    CT(7);
    ST(2);
    CT_FLUSH(32 + h, s);
    ST_FLUSH(32 + h, s);
    slot = slot == 2 ? 0 : slot + 1;
  }
}

DEVI void role_S(const Params& p, const Ctx& c, int h, unsigned cls0, unsigned cls1) {
  const int lane = c.lane, w = c.w, g = c.g, n = c.n;
  v8u W[15][3];
  sv_load_weights<false>(p, c, h, W);
  EV(1);
  const int idx = (lane & 2) ? 0xEEEE : 0x4444;
  ResW rw; rw.lo = c.lb + w * 6144 + lane * 16; rw.hi = rw.lo + 49152; PINV(rw.lo); PINV(rw.hi);
  unsigned iw = c.lb + SV_I2 + img_wr(c), ir = c.lb + SV_I2 + img_rd(c), ma = c.lb + SV_MEMI + lane * 16;
  PINV(iw); PINV(ir); PINV(ma);
  const bool tokw = w == 1 || w == 2;
  constexpr unsigned QI = SV_I1 - SV_I2;
  const float CS = 0.07216878364870322f * 1.4426950408889634f;
  bool dead = false; int have = 0;
  const unsigned hw = tokw ? w - 1 : w & 1;
  int pvm = 0;
  auto late = [&](unsigned ra, unsigned rb, unsigned pmx_v, unsigned pmm_v) __attribute__((always_inline)) {
    const v2u sv2 = sent2(); __builtin_amdgcn_raw_buffer_store_b64(sv2, c.rs, pmx_v, ra, 16); __builtin_amdgcn_raw_buffer_store_b32(sv2.x, c.rs, pmm_v, rb, 16);
    int l3 = lane; PINV(l3);
    if (l3 < 3) dma_words_sc1(p.ws, C_PROGP + ((tokw ? 0 : 96) + 3 * h + l3) * PW, c.lb + SV_SMP + w * 16);
  };
  auto stage = [&](int s) {
    if (pvm <= s) { have = 0; wait_prog(c.rs, C_PROGP + ((tokw ? 0 : 96) + 3 * h) * PW, 3, s, have, dead, lane); }
    const u8* src = p.ws + (tokw ? O_QX : O_KX) + (size_t)(s * 32 + h) * 12288 + hw * 6144;
    const unsigned dst = c.lb + (tokw ? SV_QX : SV_KX) + hw * 6144;
    dma3_sc1(src, lane * 16, dst); dma3_sc1(src + 3072, lane * 16, dst + 3072);
  };
  f4 a0 = zero4(), a1 = zero4();
  auto token_scores = [&]() __attribute__((always_inline)) {
    int lane = c.lane; PINV(lane);
    const unsigned qa = c.lb + SV_QX + hw * 6144 + lane * 16, ka = c.lb + SV_KX + lane * 16;
    v4u Q[6], K0[6], K1[6];
    a0 = zero4(); a1 = zero4();
#pragma unroll
    for (int u = 0; u < 6; ++u) { Q[u] = lr16(qa + u * 1024); K0[u] = lr16(ka + u * 1024); K1[u] = lr16(ka + 6144 + u * 1024); }
#pragma unroll
    for (int u = 0; u < 6; ++u) { mfma_v(a0, K0[u], Q[u]); mfma_v(a1, K1[u], Q[u]); }
    asm volatile("s_nop 7" : "+v"(a0), "+v"(a1));
  };
  lw16(c.lb + SV_SMP + w * 16, (v4u){0, 0, 0, 0});
  stage(0);
  lw4(c.lb + SV_FLG + hw * 4, 0u);
  WAITV(0);
  bar();
  if (tokw) token_scores();
  EV(2);
  const unsigned hn = n >> 3, pc = n & 7;
  unsigned pmx_v = (pc >> 2) * 256 + (4 * g + hn + 2 * ((pc >> 1) & 1)) * 16 + (pc & 1) * 8;
  unsigned pmm_v = (pc >> 2) * 64 + (2 * g + hn) * 8 + ((pc >> 1) & 1) * 4;
  const unsigned pmx_s = L_P + w * L_P_REP + (h >> 3) * 5120 + 1024 + (h & 7) * 512, pmm_s = L_P + w * L_P_REP + (h >> 3) * 5120 + (h & 7) * 128;
  unsigned kb = c.lb + SV_KX + (w & 1) * 6144 + lane * 16;
  unsigned sxw = c.lb + SV_SMX + 2 * g * 144 + ((((n >> 3) * 2 + ((n >> 1) & 1)) * 2 + ((n >> 2) & 1)) * 4 + (n & 1) + 2 * (w & 1)) * 4;
  unsigned sxr = c.lb + SV_SMX + (2 * g + hn) * 144 + pc * 16;
  unsigned kw = c.lb + SV_I2 + (16 * w + n) * 16 + g * 4;
  unsigned kr = c.lb + SV_I2 + (16 * g + ((lane >> 2) & 1) + 4 * ((lane >> 3) & 1) + 2 * ((lane >> 1) & 1)) * 16 + (lane & 1) * 8;
  PINV(pmx_v); PINV(pmm_v); PINV(kb); PINV(sxw); PINV(sxr); PINV(kw); PINV(kr);
  const unsigned plan = amap(L_CLK, cls0) + (40 + c.x * 2 + 1) * 128;
  unsigned planv = 0xFF000000u;
  const unsigned myhint = (h & 1) ? OOB : amap(L_CLK, cls0) + (48 + (h >> 1)) * 128;
  int slot = 0;
  ST_DECL
  for (int s = 0; s < NSEG; ++s) {
    v4u T[3][2];
#pragma unroll
    for (int t = 0; t < 3; ++t) { T[t][0] = res_h(rw, 4 * t, 0, 0); T[t][1] = res_h(rw, 4 * t, 0, 1); }
    f4 q[3], k[3], sx, sm;
#pragma unroll
    for (int t = 0; t < 3; ++t) q[t] = zero4();
    k[0] = zero4(); k[1] = zero4();
    unsigned pa = WIN + amap(slot * L_P_SLOT + pmx_s, cls1), pb = WIN + amap(slot * L_P_SLOT + pmm_s, cls1);
    const unsigned pslot = (slot == 0 ? 2 : slot - 1) * L_P_SLOT;
    unsigned ra = s > 0 ? WIN + amap(pslot + pmx_s, cls1) : OOB, rb = s > 0 ? WIN + amap(pslot + pmm_s, cls1) : OOB;
    rsrc_t rsl = c.rs;
    asm volatile("" : "+s"(pa), "+s"(pb), "+s"(ra), "+s"(rb), "+s"(rsl));
    SB();
    if (!tokw) { s_gather(c, L_MEM + slot * L_MEM_SLOT + c.x * L_MEM_REP, cls0, ma, dead, plan, planv, s, st_); }
    RAWBAR();
    ST(1);
    CT_DECL CT(0);
    v4u A[10];
#pragma unroll
    for (int j = 0; j < 5; ++j) { A[vks(j)] = lr16(ma + vks(j) * 1024); SB(); }
    v4u KB[6], AQ[3], AK[3];
    f4 xs; unsigned fv = 0; float f0 = 0.f, f1 = 0.f, g0 = 0.f, g1 = 0.f, mxx = 0.f, nmx = 0.f, lx = 0.f, x0 = 0.f, x1 = 0.f, x2 = 0.f, x3 = 0.f;
    auto ldH = [&](int t, int ci, int tile, int hf) __attribute__((always_inline)) { T[t][hf] = res_h(rw, ci, tile, hf); };
    auto fold1 = [&](f4 a, int st) __attribute__((always_inline)) {
      if (st == 0) { f0 = a[0] + a[2]; PINV(f0); }
      if (st == 1) { f1 = a[1] + a[3]; PINV(f1); }
      if (st == 2) { fv = pk(f0, f1); PINV(fv); }
    };
    sfor<0, 60>([&](auto I_) __attribute__((always_inline)) {
      constexpr int i = decltype(I_)::v;
      if constexpr (i < 30) {
        constexpr int t = i / 10, ks = vks(i % 10);
        if constexpr (res_slot(ks) >= 0) q[t] = smf(A[ks], cat(T[ks / 4][0], T[ks / 4][1]), q[t], idx); else q[t] = smf(A[ks], W[reg_idx(ks)][t], q[t], idx);
      } else {
        constexpr int t = (i - 30) / 10, ks = (i - 30) % 10, ti = ks == 3 ? (2 * t) % 3 : (2 * t + 1) % 3;
        if constexpr (res_slot(10 + ks) >= 0) k[t] = smf(A[ks], cat(T[ti][0], T[ti][1]), k[t], idx); else k[t] = smf(A[ks], W[reg_idx(10 + ks)][t], k[t], idx);
      }
      SB();
      if constexpr (i < 5) A[vks(5 + i)] = lr16(ma + vks(5 + i) * 1024);
      if constexpr (i < 20 && (i % 10 == 5 || i % 10 == 6)) ldH(0, 0, i / 10 + 1, (i % 10) & 1 ^ 1);
      if constexpr (i < 20 && (i % 10 == 7 || i % 10 == 8)) ldH(1, 4, i / 10 + 1, (i % 10) & 1 ^ 1);
      if constexpr (i >= 10 && i < 30 && (i % 10 == 0 || i % 10 == 1)) ldH(2, 8, i / 10, i & 1);
      if constexpr (i == 22) KB[0] = lr16(kb);
      if constexpr (i == 28 || i == 29) KB[i - 27] = lr16(kb + (i - 27) * 1024);
      if constexpr (i >= 31 && i < 34) KB[i - 28] = lr16(kb + (i - 28) * 1024);
      if constexpr (i == 23 || i == 24) ldH(0, 13, 0, i & 1 ^ 1);
      if constexpr (i == 26 || i == 27) ldH(1, 17, 0, i & 1);
      if constexpr (i == 30 || i == 31) ldH(2, 13, 1, i & 1);
      if constexpr (i >= 14 && i < 17) fold1(q[0], i - 14);
      if constexpr (i == 19) lw4(iw + QI, fv);
      if constexpr (i >= 22 && i < 25) fold1(q[1], i - 22);
      if constexpr (i == 25) lw4(iw + QI + 1024, fv);
      if constexpr (i == 31) { sx = zero4(); }
      if constexpr (i == 32) { fold1(q[2], 0); fold1(q[2], 1); }
      if constexpr (i == 33) fold1(q[2], 2);
      if constexpr (i == 34) lw4(iw + QI + 2048, fv);
      if constexpr (i == 36) k[2] = zero4();
      if constexpr (i == 38) bar();
      if constexpr (i >= 38 && i < 41) AQ[i - 38] = tr2(ir + QI + (i - 38) * 1024, 128);
      if constexpr (i == 41 || i == 42) ldH(0, 17, 1, i & 1 ^ 1);
      if constexpr (i >= 42 && i < 45) { SB(); sx = smf(AQ[i - 42], cat(KB[2 * (i - 42)], KB[2 * (i - 42) + 1]), sx, idx); SB(); }
      if constexpr (i == 43 || i == 44) ldH(1, 13, 2, i & 1 ^ 1);
      if constexpr (i >= 42 && i < 45) fold1(k[0], i - 42);
      if constexpr (i == 45 || i == 46) ldH(2, 17, 2, i & 1 ^ 1);
      if constexpr (i == 45) lw4(kw, fv);
      if constexpr (i == 47) { g0 = sx[0] + sx[2]; g1 = sx[1] + sx[3]; PINV(g0); PINV(g1); }
      if constexpr (i == 48) { lwf(sxw, g0); lwf(sxw + 144, g1); }
      if constexpr (i == 50) sm = zero4();
      if constexpr (i == 52) { bar(); xs = lrf4(sxr); }
      if constexpr (i == 53) AK[0] = tr2(kr, 128);
      if constexpr (i >= 53 && i < 56) fold1(k[1], i - 53);
      if constexpr (i == 56) { mxx = vmax(vmax(vmax(xs[0], xs[1]), xs[2]), xs[3]); PINV(mxx); lw4(kw + 1024, fv); }
      if constexpr (i == 57) asm volatile("v_max_f32_dpp %0, %0, %0 quad_perm:[1,0,3,2] row_mask:0xf bank_mask:0xf" : "+v"(mxx));
      if constexpr (i == 58) asm volatile("s_nop 0\n\tv_max_f32_dpp %0, %0, %0 quad_perm:[2,3,0,1] row_mask:0xf bank_mask:0xf" : "+v"(mxx));
      if constexpr (i == 59) asm volatile("s_nop 0\n\tv_max_f32_dpp %0, %0, %0 row_half_mirror row_mask:0xf bank_mask:0xf" : "+v"(mxx));
      SB();
    });
    nmx = -mxx * CS;
    x0 = fmaf(xs[0], CS, nmx); x1 = fmaf(xs[1], CS, nmx); x2 = fmaf(xs[2], CS, nmx); x3 = fmaf(xs[3], CS, nmx);
    PINV(x0); PINV(x1); PINV(x2); PINV(x3); PINV(nmx);
    SB();
    fv = fold(k[2]); PINV(fv);
    SB();
    x0 = ex2(x0); PINV(x0);
    SB();
    lw4(kw + 2048, fv);
    SB();
    mfma_v(sm, AQ[0], AK[0]);
    SB();
    x1 = ex2(x1); x2 = ex2(x2); x3 = ex2(x3); PINV(x1); PINV(x2); PINV(x3); asm volatile("" ::"v"(fv));
    SB();
    bar();
    AK[1] = tr2(kr + 1024, 128); AK[2] = tr2(kr + 2048, 128);
    SB();
    lx = (x0 + x1) + (x2 + x3); PINV(lx);
    SB();
    mfma_v(sm, AQ[1], AK[1]);
    const float sv = mfma_fold(sm, AQ[2], AK[2]);
    {
      float z = fmaf(sv, CS, nmx);
      unsigned long long big = __builtin_amdgcn_ballot_w64(!(z <= 64.f));
      float e = ex2(z);
      float ri = rcp(rowsum8(lx + e));
      PINV(ri);
      if (__builtin_expect(big != 0, 0)) {
        const float zm = rowmax8(vmax(z, 0.f));
        const float cx = ex2(-zm);
        x0 *= cx; x1 *= cx; x2 *= cx; x3 *= cx;
        e = ex2(z - zm);
        ri = rcp(rowsum8(fmaf(lx, cx, e)));
      }
      const float pm = e * ri;
      const unsigned pmm = pkpos(dppf<0xA0>(pm), dppf<0xF5>(pm));
      __builtin_amdgcn_raw_buffer_store_b64((v2u){pkpos(x0 * ri, x1 * ri), pkpos(x2 * ri, x3 * ri)}, rsl, pmx_v, pa, 16);
      __builtin_amdgcn_raw_buffer_store_b32(pmm, rsl, pmm_v, pb, 16);
      SB();
    }
    CT(7);
    if (w == 0) { ST(2); }
    RAWBAR();
    planv = plan_load(c, tokw ? OOB : plan);
    { const v4u pv = *(volatile LAS v4u*)(uintptr_t)(c.lb + SV_SMP + w * 16); pvm = __builtin_amdgcn_readfirstlane(min(min((int)pv.x, (int)pv.y), (int)pv.z)); }
    SB();
    CT_FLUSH(h, s);
    if (tokw) {
      int lane = c.lane; PINV(lane);
      const int g = lane >> 4, n = lane & 15, tt = w - 1;
      const unsigned qa = c.lb + SV_QX + tt * 6144 + lane * 16;
      v4u Q[6];
#pragma unroll
      for (int u = 0; u < 6; ++u) Q[u] = lr16(qa + u * 1024);
      f4 am = zero4();
      const int idx8 = (lane & 8) ? 0xEEEE : 0x4444;
      SB();
#pragma unroll
      for (int u = 0; u < 3; ++u) am = smf(AK[u], cat(Q[2 * u], Q[2 * u + 1]), am, idx8);
      SB();
      if (s + 1 < NSEG) stage(s + 1);
      const float MASKED = -1.0e9f / 0.07216878364870322f;
      float v[10];
#pragma unroll
      for (int r = 0; r < 4; ++r) { v[r] = (4 * g + r <= n + 16 * tt) ? a0[r] : MASKED; v[4 + r] = (16 + 4 * g + r <= n + 16 * tt) ? a1[r] : MASKED; }
      v[8] = swapadd32(am[0], am[2]); v[9] = swapadd32(am[1], am[3]);
      float m = vmax(vmax(vmax(v[0], v[1]), vmax(v[2], v[3])), vmax(vmax(v[4], v[5]), vmax(vmax(v[6], v[7]), vmax(v[8], v[9]))));
      m = -CS * x32max(x16max(m));
#pragma unroll
      for (int jj = 0; jj < 10; ++jj) v[jj] = ex2(fmaf(v[jj], CS, m));
      const float ri = rcp(x32add(x16add(((v[0] + v[1]) + (v[2] + v[3])) + ((v[4] + v[5]) + (v[6] + v[7])) + (v[8] + v[9]))));
      const unsigned xb = O_PX + s * PX_SLOT + ((h >> 3) * 2 + tt) * 10240;
      __builtin_amdgcn_raw_buffer_store_b128((v4u){pk(v[0] * ri, v[1] * ri), pk(v[2] * ri, v[3] * ri), pk(v[4] * ri, v[5] * ri), pk(v[6] * ri, v[7] * ri)}, c.rs, lane * 16, xb + (2 + (h & 7)) * 1024, 16);
      __builtin_amdgcn_raw_buffer_store_b32(pk(v[8] * ri, v[9] * ri), c.rs, ((2 * (g & 1) + (g >> 1)) * 16 + n) * 4, xb + (h & 7) * 256, 16);
      ST(2);
      late(ra, rb, pmx_v, pmm_v);
      WAITV(5);
      if (s + 1 < NSEG)
        for (unsigned it = 0; it < SPIN_CAP * 64u; ++it) {
          FENCE();
          const v2u f = *(volatile LAS v2u*)(uintptr_t)(c.lb + SV_FLG);
          if (__builtin_amdgcn_readfirstlane(min(f.x, f.y)) > (unsigned)s) break;
        }
      ST(3);
      if (s + 1 < NSEG) token_scores();
      WAITV(0);
    } else {
      if (w == 0) hint_put(c, myhint, now32(), s + 1);
      else hint_put(c, C_PROGQ + h * PW, dead ? 0x10000u : (unsigned)s, 0);
      if (s + 1 < NSEG) stage(s + 1);
      late(ra, rb, pmx_v, pmm_v);
    }
    if (s + 1 == NSEG) { RAWBAR(); if (w == 3 && lane == 0) st4(c.rs, C_PROGQ + h * PW, 0x10000u); }
    ST_FLUSH(h, s);
    slot = slot == 2 ? 0 : slot + 1;
  }
}

DEVI void role_M(const Params& p, const Ctx& c, int cc, unsigned cls0, unsigned cls1) {
  const int lane = c.lane, w = c.w, g = c.g, n = c.n;
  const int idx = (lane & 2) ? 0xEEEE : 0x4444;
  bool dead = false; int haveU = 0;
  const unsigned uoff = O_U + (cc * 32 + w * 8) * 1024 + lane * 16;
  const unsigned progU = C_PROGU + ((cc & 3) * 32 + w * 8) * PW;
  v4u U[8];
  wait_prog(c.rs, progU, 8, 0, haveU, dead, lane);
  EV(1);
#pragma unroll
  for (int k = 0; k < 8; ++k) U[k] = ld16(c.rs, uoff + k * 1024);
  unsigned rdw = c.lb + w * 512 + 2 * g * 64 + n * 4;
  unsigned rdr = c.lb + (2 * (lane >> 4) + ((lane >> 2) & 1)) * 64 + ((lane >> 3) & 1) * 32 + (lane & 3) * 8;
  unsigned l4 = lane * 4;
  PINV(rdw); PINV(rdr); PINV(l4);
  const unsigned clk = amap(L_CLK, cls0);
  int pv = 0; unsigned pvo = progU + (lane & 7) * PW; PINV(pvo);
  int qq = 126, kk = 72; unsigned f1p = 0;
  bool locked = false, lockedp = false;
  unsigned f1g = 0, f2p = 0, tv = 0, tvo = clk + ((lane < 40 || lane >= 48) ? lane * 128 : lane == 47 ? 4 : 0);
  PINV(tvo);
  unsigned pm, po[4], ybn, mo_n = amap(L_MEM + w * L_MEM_REP + cc * 256, cls0);
  auto next_addr = [&](int sl) __attribute__((always_inline)) {
    ybn = (sl < 2 ? amap(L_Y01 + sl * L_Y_SLOT, cls0) : WIN + cls1) + cc * 16384 + w * 2048;
    asm volatile("" : "+s"(ybn));
    const unsigned pb = L_P + sl * L_P_SLOT + c.x * L_P_REP + w * 5120;
    pm = WIN + amap(pb, cls1) + (4 * (g >> 1) + ((n >> 1) & 1)) * 128 + (g & 1) * 64 + (2 * (n >> 2) + (n & 1)) * 8;
#pragma unroll
    for (int k = 0; k < 4; ++k) { po[k] = WIN + amap(pb + 1024 + k * 1024, cls1) + lane * 16; PINV(po[k]); }
    PINV(pm);
  };
  next_addr(0);
  unsigned f1 = 0, f2 = 0;
  int slot = 0;
  ST_DECL
  for (int s = 0; s + 1 < NSEG; ++s) {
    const unsigned yb = ybn, mo_c = mo_n;
    const int nslot = slot == 2 ? 0 : slot + 1;
    mo_n = amap(L_MEM + w * L_MEM_REP + cc * 256, cls0) + nslot * (2 * L_MEM_SLOT);
    asm volatile("" : "+s"(mo_n));
    v4u Y0, Y1;
    unsigned cnt = dead ? 1u : SPIN_CAP;
    auto ldY = [&]() __attribute__((always_inline)) {
#pragma unroll
      for (int k = 0; k < 4; ++k) { Y0[k] = __builtin_amdgcn_raw_buffer_load_b32(c.rs, l4 + k * 256, yb, 16); Y1[k] = __builtin_amdgcn_raw_buffer_load_b32(c.rs, l4 + 1024 + k * 256, yb, 16); }
    };
    if (w == 0 && s > 0) wait_time(f2 - c.ka);
    RAWBAR();
    ldY();
    SB();
    if (w == 0 && s > 0) wait_time(f2);
    RAWBAR();
    ST(0);
    auto body = [&]() __attribute__((always_inline)) {
      v2u m0 = ld8(c.rs, pm), m1 = ld8(c.rs, pm + 256);
      v4u P[4];
#pragma unroll
      for (int k = 0; k < 4; ++k) P[k] = ld16(c.rs, po[k]);
      f4 acc = ZERO4; PINV(acc);
      SB();
      unsigned m = chk(chk(0u, Y0), Y1);
      SB();
      acc = smf((v4u){m0.x, m1.x, m0.y, m1.y}, cat(Y0, Y1), acc, idx);
      SB();
      m = chk(chk(m, m0), m1);
      SB();
#pragma unroll
      for (int k = 0; k < 4; ++k) { acc = smf(P[k], cat(U[2 * k], U[2 * k + 1]), acc, idx); SB(); m = chk(m, P[k]); SB(); }
      const bool r = all_valid(m);
      lwf(rdw, acc[0] + acc[2]); lwf(rdw + 64, acc[1] + acc[3]);
      return r;
    };
    bool ok = body();
    if (__builtin_expect(!ok, 0)) {
#pragma clang loop unroll(disable)
      do {
        FENCE();
        ldY();
        ok = body();
      } while (!ok && --cnt);
    }
    ST(1);
    WAITV(0);
    bar();
    {
      f2_t v = (lrf2(rdr) + lrf2(rdr + 512)) + (lrf2(rdr + 1024) + lrf2(rdr + 1536));
      __builtin_amdgcn_raw_buffer_store_b32(pk(v.x, v.y), c.rs, l4, mo_n, 16);
    }
    SB();
    ST(2);
    if (!ok) dead = true;
    unsigned tm = 0;
    auto rowmax = [&](unsigned v, unsigned ref) __attribute__((always_inline)) {
      int d = (int)((v - ref) << 8) >> 8;
      d = max(d, dppi<0xB1>(d)); d = max(d, dppi<0x4E>(d)); d = max(d, dppi<0x124>(d)); d = max(d, dppi<0x128>(d));
      return d;
    };
    if (w == 0) {
      tm = now32();
      unsigned z; asm volatile("v_mov_b32 %0, 0" : "=v"(z));
      __builtin_amdgcn_raw_buffer_store_b64((v2u){(tm & 0xFFFFFFu) | ((unsigned)(s + 1) << 24), (f1 << 16) | ((unsigned)qq << 8) | (unsigned)kk}, c.rs, z, clk + cc * 128, 16);
    }
    FENCE();
    if (__builtin_amdgcn_ballot_w64(pv <= s + 1) != 0) { pv = (int)ld4(c.rs, pvo); PINV(pv); }
    const bool uready = __builtin_amdgcn_ballot_w64(pv <= s + 1) == 0;
    {
      const unsigned uo = uready ? uoff + (s + 1) * (40 * 32768) : OOB;
#pragma unroll
      for (int k = 0; k < 8; ++k) U[k] = ld16(c.rs, uo + k * 1024);
    }
    FENCE();
    next_addr(nslot);
    if (w == 0) {
      if (s > 1 && cc == 0) {
        const int d = rowmax(tv, f2p);
        const int tpx = __builtin_amdgcn_readlane(d, 48), tmx = max(max(__builtin_amdgcn_readlane(d, 0), __builtin_amdgcn_readlane(d, 16)), __builtin_amdgcn_readlane(d, 32));
        const int mq = tpx + (int)(f2p - f1p) + c.q, mk = tmx + c.k1;
        if (lockedp && mq < qq + 16 && mq > qq - 64) qq = min(max(mq, qq - c.dec), qq + c.kd);
        if (tpx <= 2 && mk < kk + 16) kk = min(max(mk, kk - c.dec), kk + c.kd);
        qq = min(max(qq, 60), 250); kk = min(max(kk, 30), 250);
      }
      wait_time(max_t(f1, tm + 10) + c.kb);
    }
    {
      unsigned tvo2 = w == 0 ? tvo : OOB, ro = yb + lane * 16, yc = O_YC + (s * 40 + cc) * 8192 + w * 2048 + lane * 16;
      unsigned pgo = lane == 0 ? C_PROGY + (w * 40 + cc) * PW : OOB, pgv = dead ? 0x10000u : (unsigned)s;
      v4u ff = sent4();
      PINV(tvo2); PINV(ro); PINV(ff); PINV(yc); PINV(pgo); PINV(pgv);
      SB();
      RAWBAR();
      asm volatile("buffer_load_dword %0, %1, %2, 0 offen sc1" : "=v"(tv) : "v"(tvo2), "s"(c.rs) : "memory");
      pv = (int)ld4(c.rs, pvo);
      st16(c.rs, yc, Y0); st16(c.rs, yc + 1024, Y1);
      st4(c.rs, pgo, pgv);
      __builtin_amdgcn_raw_buffer_store_b32(ff.x, c.rs, l4, mo_c, 16);
      st16(c.rs, ro, ff); st16(c.rs, ro + 1024, ff);
      SB();
      asm volatile("s_waitcnt vmcnt(7)" : "+v"(tv)::"memory");
      SB();
    }
    if (w == 0) {
      constexpr unsigned long long MM = 0xFFFFFFFFFFull;
      int uq = qq, uk = kk;
      auto adopt = [&]() __attribute__((always_inline)) {
        const unsigned ms = __builtin_amdgcn_readlane(tv, 47);
        f1 = tm + (unsigned)((int)(((ms >> 16) - tm) << 16) >> 16); uq = (ms >> 8) & 255; uk = ms & 255;
        tv = lane == 47 ? __builtin_amdgcn_readlane(tv, 0) : tv;
      };
      lockedp = locked; f2p = f2;
      bool done = false;
      if ((__builtin_amdgcn_ballot_w64((tv >> 24) != (unsigned)(s + 1)) & MM) == 0) {
        adopt();
        if (s > 0 && (__builtin_amdgcn_ballot_w64(((int)((tv - f1) << 8) >> 8) > 3) & MM) == 0) { locked = true; f2 = f1 + uq; done = true; }
      } else {
        int tries = 64;
        while ((__builtin_amdgcn_ballot_w64((tv >> 24) - (unsigned)(s + 1) > 1u) & MM) != 0 && --tries >= 0) {
          __builtin_amdgcn_s_sleep(4);
          FENCE();
          tv = ld4(c.rs, tvo);
          PINV(tv);
        }
        if ((__builtin_amdgcn_ballot_w64((tv >> 24) != (unsigned)(s + 1)) & MM) != 0) { f2 = now32(); locked = false; lockedp = false; f2p -= 4096; done = true; }
        else adopt();
      }
      if (!done) {
        const int d = rowmax(tv, tm);
        const int tmx = max(max(__builtin_amdgcn_readlane(d, 0), __builtin_amdgcn_readlane(d, 16)), __builtin_amdgcn_readlane(d, 32));
        if (s > 0 && (int)(f1 - tm) - tmx >= -3) { locked = true; f2 = f1 + uq; }
        else { f2 = tm + tmx + (locked ? 32 : 64) + uq; locked = false; }
      }
      f1p = f1g; f1g = f1;
      f1 = f2 + uk;
      if (cc % 10 < 2) hint_put(c, clk + (40 + c.x * 2 + cc % 10) * 128, f1, s + 2);
    }
    if (!uready) {
      haveU = 0; wait_prog(c.rs, progU, 8, s + 1, haveU, dead, lane);
#pragma unroll
      for (int k = 0; k < 8; ++k) U[k] = ld16(c.rs, uoff + (s + 1) * (40 * 32768) + k * 1024);
      pv = haveU;
    }
    slot = nslot;
    ST_FLUSH(64 + cc, s);
  }
  {
    if (w == 0) wait_time(f2 - c.ka);
    RAWBAR();
    v4u Y0, Y1; unsigned cnt = dead ? 1u : SPIN_CAP;
    do {
      FENCE();
#pragma unroll
      for (int k = 0; k < 4; ++k) { Y0[k] = __builtin_amdgcn_raw_buffer_load_b32(c.rs, l4 + k * 256, ybn, 16); Y1[k] = __builtin_amdgcn_raw_buffer_load_b32(c.rs, l4 + 1024 + k * 256, ybn, 16); }
    } while (!all_valid(chk(chk(0u, Y0), Y1)) && --cnt);
    const unsigned yc = O_YC + ((NSEG - 1) * 40 + cc) * 8192 + w * 2048 + lane * 16;
    st16(c.rs, yc, Y0); st16(c.rs, yc + 1024, Y1);
    WAITV(0);
    if (lane == 0) st4(c.rs, C_PROGY + (w * 40 + cc) * PW, 0x10000u);
    ST(2); ST_FLUSH(64 + cc, NSEG - 1);
  }
}

DEVI void role_T(const Params& p, const Ctx& c, int c0) {
  const int lane = c.lane, w = c.w, g = c.g, n = c.n;
  bool dead = false; int haveA = 0, haveB = 0;
  const int cw = c0 + (w >> 1), rt = w & 1;
  const unsigned progA = (lane < 8 ? C_PROGQ + (w * 8 + lane) * PW : lane < 10 ? C_PROGY + (w * 40 + c0 + lane - 8) * PW
                                   : C_PROGG + (((lane & 1) * 8 + cw / 5) * 4 + (((cw % 5) * 2 + rt) & 3)) * PW) - PW * (unsigned)lane;
  const unsigned progU = C_PROGU + (((c0 & 3) + ((lane >> 3) & 1)) * 32 + w * 8 + (lane & 7)) * PW - PW * (unsigned)lane;
  unsigned ub = O_U + (c0 * 32 + w * 8) * 1024 + lane * 16;
  unsigned svo = progA + PW * (unsigned)lane;
  PINV(ub); PINV(svo);
  unsigned* tick = (unsigned*)(p.ws + C_TICK + (c0 >> 1) * PW);
  const unsigned tk = c.lb + 32768;
  bar();
  if (w == 0 && lane == 0) { const unsigned t = __hip_atomic_fetch_add(tick, 1u, __ATOMIC_RELAXED, __HIP_MEMORY_SCOPE_AGENT); lw4(tk, t); }
  bar();
  int s = __builtin_amdgcn_readfirstlane(*(LAS unsigned*)(uintptr_t)tk), sv = 0;
  if (s >= NSEG) return;
  v4u U[2][8];
  wait_prog(c.rs, progU, 64, s, haveB, dead, lane);
#pragma unroll
  for (int q = 0; q < 2; ++q)
#pragma unroll
    for (int k = 0; k < 8; ++k) U[q][k] = __builtin_amdgcn_raw_buffer_load_b128(c.rs, ub + (k & 3) * 1024, s * (40 * 32768) + q * 32768 + (k >> 2) * 4096, 16);
  for (int it = 1; s < NSEG; ++it) {
    if (__builtin_amdgcn_ballot_w64(sv <= s) != 0) { haveA = 0; wait_prog(c.rs, progA, 64, s, haveA, dead, lane); }
    if (w == 0 && lane == 0) { const unsigned t = __hip_atomic_fetch_add(tick, 1u, __ATOMIC_RELAXED, __HIP_MEMORY_SCOPE_AGENT); lw4(tk + (it & 1) * 4, t); }
    v4u X[20], Y[2][2];
    const unsigned xb = O_PX + s * PX_SLOT + w * 20480 + lane * 16, xm = O_PX + s * PX_SLOT + w * 20480 + lane * 4;
#pragma unroll
    for (int k = 2; k < 10; ++k) { X[k] = ld16(c.rs, xb + k * 1024); X[10 + k] = ld16(c.rs, xb + (10 + k) * 1024); }
#pragma unroll
    for (int k = 0; k < 2; ++k) {
#pragma unroll
      for (int u = 0; u < 4; ++u) { X[k][u] = ld4(c.rs, xm + k * 1024 + u * 256); X[10 + k][u] = ld4(c.rs, xm + (10 + k) * 1024 + u * 256); }
#pragma unroll
      for (int q = 0; q < 2; ++q) Y[q][k] = ld16(c.rs, O_YC + (s * 40 + c0 + q) * 8192 + w * 2048 + k * 1024 + lane * 16);
    }
    const size_t orow = (size_t)(s * 32 + rt * 16 + n) * 1280 + cw * 32 + g * 8;
    const v2u gv = ld8(c.rs, O_G + ((s * 40 + cw) * 2 + rt) * 512 + lane * 8);
    const v2u xv = *(const v2u*)(p.x + orow);
    SB();
    bar();
    const int sn = __builtin_amdgcn_readfirstlane(*(LAS unsigned*)(uintptr_t)(tk + (it & 1) * 4));
    if (sn < NSEG) wait_prog(c.rs, progU, 64, sn, haveB, dead, lane);
    const unsigned rb = c.lb + (it & 1) * 16384;
    const unsigned un = sn < NSEG ? sn * (40 * 32768) : OOB / 2;
    f4 o[4] = {ZERO4, ZERO4, ZERO4, ZERO4};
#pragma unroll
    for (int k = 0; k < 8; ++k) {
#pragma unroll
      for (int q = 0; q < 2; ++q) {
        o[2 * q] = mfma(U[q][k], X[2 + k], o[2 * q]); o[2 * q + 1] = mfma(U[q][k], X[12 + k], o[2 * q + 1]);
        SB();
        U[q][k] = __builtin_amdgcn_raw_buffer_load_b128(c.rs, ub + (k & 3) * 1024, un + q * 32768 + (k >> 2) * 4096, 16);
        SB();
      }
    }
    sv = (int)ld4(c.rs, svo);
    SB();
#pragma unroll
    for (int k = 0; k < 2; ++k)
#pragma unroll
      for (int q = 0; q < 2; ++q) { o[2 * q] = mfma(Y[q][k], X[k], o[2 * q]); o[2 * q + 1] = mfma(Y[q][k], X[10 + k], o[2 * q + 1]); }
#pragma unroll
    for (int q = 0; q < 4; ++q) lwf4(rb + (w * 4 + q) * 1024 + lane * 16, o[q]);
    bar();
    {
      const unsigned a = rb + w * 1024 + lane * 16;
      f4 v = (lrf4(a) + lrf4(a + 4096)) + (lrf4(a + 8192) + lrf4(a + 12288));
      const unsigned s01 = pk(v[0], v[1]), s23 = pk(v[2], v[3]);
      const unsigned t01 = pk(blo(s01) * blo(gv.x), bhi(s01) * bhi(gv.x)), t23 = pk(blo(s23) * blo(gv.y), bhi(s23) * bhi(gv.y));
      v2u ov = {pk(blo(xv.x) + blo(t01), bhi(xv.x) + bhi(t01)), pk(blo(xv.y) + blo(t23), bhi(xv.y) + bhi(t23))};
      if (dead) ov = (v2u){0x7FC07FC0u, 0x7FC07FC0u};
      *(v2u*)(p.out + orow) = ov;
    }
    s = sn;
  }
}

DEVI void role_F(const Ctx& c, int j, unsigned cls0) {
  const unsigned plan = amap(L_CLK, cls0) + (40 + c.x * 2 + j) * 128;
  int last = 0;
  for (int it = 0; it < 4000 && last < NSEG - 1; ++it) {
    const int t = window_wait(c, plan, last);
    if (t == 0 || t + 1 >= NSEG) continue;
    const unsigned ko = O_QX + j * QKV_B + ((t + 1) * 32 + c.x * 8) * 12288 + c.w * 24576 + c.lane * 128;
    const unsigned uo = O_U + ((t + 1) * 40 + c.x * 10) * 32768 + (j * 4 + c.w) * 40960 + c.lane * 128;
    unsigned d[8];
#pragma unroll
    for (int k = 0; k < 8; ++k) d[k] = ld4(c.rs, k < 3 ? ko + k * 8192 : uo + (k - 3) * 8192);
#pragma unroll
    for (int k = 0; k < 8; ++k) asm volatile("" ::"v"(d[k]));
  }
}

DEVI void hold(unsigned t0, int d) { while ((int)(now32() - t0) < d) __builtin_amdgcn_s_sleep(8); }
__global__ void __launch_bounds__(256, 1) k_main(Params p, int mask, int tune, int stag) {
  extern __shared__ __attribute__((aligned(16))) u8 lds[];
  Ctx c;
  c.rs = __builtin_amdgcn_make_buffer_rsrc(p.ws, 0, RS_BYTES, 0x00020000);
  c.lb = (unsigned)(uintptr_t)(LAS u8*)lds;
  c.lane = threadIdx.x & 63; c.w = __builtin_amdgcn_readfirstlane(threadIdx.x >> 6);
  c.g = c.lane >> 4; c.n = c.lane & 15;
  c.x = blockIdx.x & 3; c.kw = (mask >> 8) & 255;
  c.k1 = (tune & 255) - 16; c.q = ((tune >> 8) & 255) - 16; c.dec = (mask >> 16) & 255; c.kd = (mask >> 24) & 127; c.ka = (tune >> 16) & 255; c.kb = (tune >> 24) & 255;
  const int b = blockIdx.x, x = b & 7, r = b >> 3;
  EV(0);
  const unsigned tstart = now32();
  const int d1 = (stag & 255) * 10, d2 = ((stag >> 8) & 255) * 10, d3 = ((stag >> 16) & 255) * 10, d0 = 0;
  c.kv = (stag >> 24) & 255;
  if (x < 4 && r < 28) {
    if (!(mask & 4)) return;
    unsigned cls[2];
#pragma unroll
    for (int wi = 0; wi < 2; ++wi) {
      const unsigned* q = (const unsigned*)(p.ws + C_CLS) + wi * 16;
      unsigned m0 = q[0], m1 = q[8];
#pragma unroll
      for (int u = 1; u < 8; ++u) { m0 = min(m0, q[u]); m1 = min(m1, q[8 + u]); }
      cls[wi] = m1 < m0 ? 8192u : 0u;
    }
    const unsigned cls0 = cls[0], cls1 = cls[1];
    if (r >= 8 && r < 16) hold(tstart, d1);
    if (r < 8) role_S(p, c, x * 8 + r, cls0, cls1);
    else if (r < 16) role_V(p, c, x * 8 + r - 8, cls0, cls1);
    else if (r < 26) role_M(p, c, x * 10 + r - 16, cls0, cls1);
    else if (!(mask & 64)) role_F(c, r - 26, cls0);
    EV(3);
    return;
  }
  const int i = x >= 4 ? r * 4 + (x - 4) : 128 + (r - 28) * 4 + x;
  hold(tstart, (i < 14 || (i >= 128 && i < 138)) ? d2 : i < 94 ? d0 : (i < 110 || i >= 138) ? d3 : 0);
  if (i < 62 || (i >= 128 && i < 138)) { if (mask & 1) role_P<false>(p, c, (i < 62 ? i + 10 : i - 128) * 4); }
  else if (i < 94) { if (mask & 2) role_U(p, c, i - 62); }
  else if (i < 110) { if (mask & 8) role_G(p, c, (i - 94) >> 1, (i - 94) & 1); }
  else if (i >= 138) { if (mask & 16) role_P<true>(p, c, 288 + (i - 138) * 4); }
  EV(2);
  if ((mask & 32) && (i < 30 || (i >= 110 && i < 128))) role_T(p, c, (i >= 110 ? i - 110 : i < 12 ? 18 + (i & 1) : i - 12) * 2);
  EV(3);
}

#define N_LAUNCH 1

extern "C" void kernel_launch(void* const* d_in, const int* in_sizes, int n_in, void* d_out, int out_size, void* d_ws,
                              size_t ws_size, hipStream_t stream) {
  Params p;
  p.x = (const u8*)d_in[0]; p.mem0 = (const u8*)d_in[1];
  p.wq = (const u8*)d_in[2]; p.wk = (const u8*)d_in[3]; p.wv = (const u8*)d_in[4]; p.wo = (const u8*)d_in[5];
  p.w1 = (const u8*)d_in[8]; p.b1 = (const u8*)d_in[9]; p.w2 = (const u8*)d_in[10]; p.b2 = (const u8*)d_in[11];
  p.out = (u8*)d_out;
  p.ws = (u8*)(((uintptr_t)d_ws + WIN - 1) & ~(uintptr_t)(WIN - 1));
  hipFuncSetAttribute((const void*)k_main, hipFuncAttributeMaxDynamicSharedMemorySize, LDS_BYTES);
  int tune = 20 | (15 << 8) | (14 << 16) | (8 << 24);
  int stag = 0 | (30 << 8) | (130 << 16) | (0 << 24);
  int mask = 191 | (12 << 8) | (2 << 16) | (6 << 24);
  k_pre<<<192, 640, 0, stream>>>(p);
  k_main<<<256, 256, LDS_BYTES, stream>>>(p, mask, tune, stag);
}
```
